```python
import torch

def _softmax(x, axis=-1):
    return torch.softmax(x, dim=axis)
def _gelu(x):
    return torch.nn.functional.gelu(x, approximate="tanh")
def _silu(x):
    return torch.nn.functional.silu(x)
def _sigmoid(x):
    return torch.sigmoid(x)
def _layernorm(x, axis=-1, eps=1e-5):
    m = x.mean(dim=axis, keepdim=True)
    v = x.var(dim=axis, keepdim=True, unbiased=False)
    return (x - m) / torch.sqrt(v + eps)
def _scatter_add(tgt, idx, src, axis=0):
    return tgt.clone().index_add_(axis, idx, src)
def _segment_sum(src, seg, n):
    out = torch.zeros((n,) + src.shape[1:], dtype=src.dtype)
    return out.index_add_(0, seg, src)
def _pad(x, pw):
    flat = tuple(v for pair in reversed(pw) for v in pair)
    return torch.nn.functional.pad(x, flat)

kernel_name = 'standalone__linear_kernel_performer__prompt'

def setup_inputs():

    _w = (lambda a: (lambda t: t.to(torch.bfloat16) if t.is_floating_point() else t)(torch.as_tensor(a)))
    B, H, S, D = 1, 32, 2048, 128
    M = D
    return {
        "q": _w((lambda *s: torch.randn(*s, dtype=torch.bfloat16) * 0.05)(B, H, S, D)),
        "k": _w((lambda *s: torch.randn(*s, dtype=torch.bfloat16) * 0.8)(B, H, S, D)),
        "v": _w((lambda *s: torch.randn(*s, dtype=torch.bfloat16) * 0.8)(B, H, S, D)),
        "proj": _w((lambda *s: torch.randn(*s, dtype=torch.bfloat16) * 0.8)(M, D)),
    }

def reference(q, k, v, proj):
    B, H, S, D = q.shape
    M = proj.shape[0]
    scale = D ** -0.25
    qs = q * scale
    ks = k * scale
    qw = torch.einsum("bhsd,md->bhsm", qs, proj)
    kw = torch.einsum("bhsd,md->bhsm", ks, proj)
    qn = torch.einsum("bhsd,bhsd->bhs", qs, qs)[..., None] * 0.5
    kn = torch.einsum("bhsd,bhsd->bhs", ks, ks)[..., None] * 0.5
    inv_sqrt_m = M ** -0.5
    phi_q = torch.exp(qw - qn) * inv_sqrt_m
    phi_k = torch.exp(kw - kn) * inv_sqrt_m
    kv = torch.einsum("bhsm,bhsd->bhmd", phi_k, v)
    ksum = torch.einsum("bhsm->bhm", phi_k)
    num = torch.einsum("bhsm,bhmd->bhsd", phi_q, kv)
    den = torch.einsum("bhsm,bhm->bhs", phi_q, ksum)[..., None] + 1e-6
    return num / den
```

```cpp
#include <hip/hip_runtime.h>
#include <hip/hip_bf16.h>
#include <stdint.h>

typedef __bf16 bf16x2 __attribute__((ext_vector_type(2)));
typedef __bf16 bf16x8 __attribute__((ext_vector_type(8)));
typedef short s16x4 __attribute__((ext_vector_type(4)));
typedef short s16x8 __attribute__((ext_vector_type(8)));
typedef float f32x2 __attribute__((ext_vector_type(2)));
typedef float f32x16 __attribute__((ext_vector_type(16)));
typedef unsigned u32x2 __attribute__((ext_vector_type(2)));
typedef unsigned u32x4 __attribute__((ext_vector_type(4)));
#define LAS __attribute__((address_space(3)))
#define GAS __attribute__((address_space(1)))
#define DI __device__ __forceinline__
typedef GAS unsigned long long gu64;

#define NTL " nt"
#define STAMP 0

constexpr int kS = 2048;
constexpr float kC1 = 0.29730177875068026f * 1.4426950408889634f;
constexpr float kCN = -0.5f * 0.08838834764831845f * 1.4426950408889634f;
constexpr float kCB = -3.5f;

constexpr unsigned WS_SYNC = 0;
constexpr unsigned WS_KS = 0x10000;
constexpr unsigned WS_F = 0x40000;
constexpr unsigned WS_P = 0x200000;
constexpr unsigned WS_STAMP = 0x1000000;
constexpr unsigned WS_BYTES = 0x1100000;

constexpr unsigned L_PROJ = 0;
constexpr unsigned L_KV = 32768;
constexpr unsigned L_KS = 131072;
constexpr unsigned L_BYTES = 131072 + 256;

constexpr unsigned kSpinCap = 1u << 20;

DI f32x16 mfma(bf16x8 a, bf16x8 b, f32x16 c) { return __builtin_amdgcn_mfma_f32_32x32x16_bf16(a, b, c, 0, 0, 0); }
DI unsigned pack2(float lo, float hi) { f32x2 f = {lo, hi}; return __builtin_bit_cast(unsigned, __builtin_convertvector(f, bf16x2)); }
DI float bflo(unsigned u) { return __uint_as_float(u << 16); }
DI float bfhi(unsigned u) { return __uint_as_float(u & 0xffff0000u); }
DI float dot2(unsigned u, float acc) { bf16x2 a = __builtin_bit_cast(bf16x2, u); return __builtin_amdgcn_fdot2_f32_bf16(a, a, acc, false); }
DI float sumsq(u32x4 v, float acc) { acc = dot2(v[0], acc); acc = dot2(v[1], acc); acc = dot2(v[2], acc); return dot2(v[3], acc); }
DI float halfsum(float x) { auto r = __builtin_amdgcn_permlane32_swap(__float_as_uint(x), __float_as_uint(x), false, false); return __uint_as_float(r[0]) + __uint_as_float(r[1]); }
DI bf16x8 asbf(u32x4 v) { return __builtin_bit_cast(bf16x8, v); }
DI u32x4 pack8(const f32x16& x, int s) {
    u32x4 p; p[0] = pack2(x[8 * s], x[8 * s + 1]); p[1] = pack2(x[8 * s + 2], x[8 * s + 3]); p[2] = pack2(x[8 * s + 4], x[8 * s + 5]); p[3] = pack2(x[8 * s + 6], x[8 * s + 7]); return p;
}
DI void bar() { asm volatile("s_waitcnt lgkmcnt(0)\n\ts_barrier" ::: "memory"); }
DI unsigned swz(unsigned row) { return ((row & 3) << 2) | ((row >> 2) & 3); }
DI void stamp(char* ws, int idx, int lane) {
}
DI void poll8(const gu64* line, unsigned long long tag, int lane) {
    for (unsigned spins = 0; spins < kSpinCap; ++spins) {
        unsigned long long x = __hip_atomic_load(line + (lane & 7), __ATOMIC_RELAXED, __HIP_MEMORY_SCOPE_AGENT);
        if (__all(x == tag)) break;
        __builtin_amdgcn_s_sleep(1);
    }
    asm volatile("" ::: "memory");
}

#define STR2(x) #x
#define STR(x) STR2(x)
#define DMA_KV(st) \
    "s_add_u32 m0, %[ldsw], " STR(st) "*16384\n\t" \
    "s_nop 0\n\t" \
    "global_load_lds_dwordx4 %[vo0], %[kb] offset:0" NTL "\n\t" \
    "global_load_lds_dwordx4 %[vo1], %[kb] offset:1024" NTL "\n\t" \
    "s_add_u32 m0, %[ldsw], " STR(st) "*16384+8192\n\t" \
    "s_nop 0\n\t" \
    "global_load_lds_dwordx4 %[vo0], %[vb] offset:0" NTL "\n\t" \
    "global_load_lds_dwordx4 %[vo1], %[vb] offset:1024" NTL "\n\t" \
    "v_add_u32 %[vo0], 0x2000, %[vo0]\n\t" \
    "v_add_u32 %[vo1], 0x2000, %[vo1]\n\t"
#define QLD(r, vq, off) "global_load_dwordx4 %[" #r "], %[" #vq "], %[qb] offset:" STR(off) NTL "\n\t"
#define QG0 QLD(q0, vq0, 0) QLD(q1, vq0, 32) QLD(q2, vq0, 64) QLD(q3, vq0, 96)
#define QG1 QLD(q4, vq0, 128) QLD(q5, vq0, 160) QLD(q6, vq0, 192) QLD(q7, vq0, 224)
#define QG2 QLD(q8, vq1, 0) QLD(q9, vq1, 32) QLD(q10, vq1, 64) QLD(q11, vq1, 96)
#define QG3 QLD(q12, vq1, 128) QLD(q13, vq1, 160) QLD(q14, vq1, 192) QLD(q15, vq1, 224)
#define WB(n) "s_waitcnt vmcnt(" STR(n) ")\n\ts_barrier\n\t"

__global__ __launch_bounds__(512, 2) void performer_kernel(const char* __restrict__ gq, const char* __restrict__ gk, const char* __restrict__ gv,
                                                           const char* __restrict__ gproj, char* __restrict__ gout, char* __restrict__ ws) {
    __shared__ __attribute__((aligned(16))) char lds[L_BYTES];
    const int lane = threadIdx.x & 63, r = lane & 31, h = lane >> 5;
    const int wv = __builtin_amdgcn_readfirstlane(threadIdx.x >> 6), w = wv & 3;
    const int b = blockIdx.x, head = (b & 7) * 4 + (b >> 6), member = (b >> 3) & 7;
    const size_t rowbase = ((size_t)head * kS + member * 256) * 256;
    stamp(ws, 0, lane);

    if (wv >= 4) {
        const unsigned ldsbase = (unsigned)(size_t)(LAS char*)lds;
        const unsigned ldsw = __builtin_amdgcn_readfirstlane(ldsbase + w * 2048);
        const unsigned lr = lane >> 4, sl = lane & 15;
        unsigned vo0 = w * 2048 + lr * 256 + 16 * (sl ^ ((lr << 2) | ((2 * w) & 3)));
        unsigned vo1 = w * 2048 + lr * 256 + 16 * (sl ^ ((lr << 2) | ((2 * w + 1) & 3)));
        const unsigned vq0 = w * 16384 + r * 256 + h * 16, vq1 = vq0 + 8192;
        const char* kb = gk + rowbase; const char* vb = gv + rowbase; const char* qb = gq + rowbase;
        u32x4 q0, q1, q2, q3, q4, q5, q6, q7, q8, q9, q10, q11, q12, q13, q14, q15; unsigned keep;
        asm volatile("s_mov_b32 %[keep], m0\n\t"
                     DMA_KV(0) DMA_KV(1) DMA_KV(2) DMA_KV(3) DMA_KV(4) DMA_KV(5)
                     WB(20) DMA_KV(6) WB(20) DMA_KV(7) WB(20) QG0 WB(20) QG1 WB(20) QG2 WB(20) QG3 WB(20) WB(16)
                     "s_waitcnt vmcnt(0)\n\ts_mov_b32 m0, %[keep]"
                     : [keep] "=&s"(keep), [vo0] "+v"(vo0), [vo1] "+v"(vo1),
                       [q0] "=&v"(q0), [q1] "=&v"(q1), [q2] "=&v"(q2), [q3] "=&v"(q3), [q4] "=&v"(q4), [q5] "=&v"(q5), [q6] "=&v"(q6), [q7] "=&v"(q7),
                       [q8] "=&v"(q8), [q9] "=&v"(q9), [q10] "=&v"(q10), [q11] "=&v"(q11), [q12] "=&v"(q12), [q13] "=&v"(q13), [q14] "=&v"(q14), [q15] "=&v"(q15)
                     : [ldsw] "s"(ldsw), [kb] "s"(kb), [vb] "s"(vb), [qb] "s"(qb), [vq0] "v"(vq0), [vq1] "v"(vq1)
                     : "memory", "scc");
        stamp(ws, 1, lane);
        const u32x4 qf[2][8] = {{q0, q1, q2, q3, q4, q5, q6, q7}, {q8, q9, q10, q11, q12, q13, q14, q15}};
        f32x16 cin[2];
#pragma unroll
        for (int u = 0; u < 2; ++u) {
            float n = 0.f;
#pragma unroll
            for (int t = 0; t < 8; ++t) n = sumsq(qf[u][t], n);
            const float c = halfsum(n) * kCN + kCB;
#pragma unroll
            for (int i = 0; i < 16; ++i) cin[u][i] = c;
        }
        u32x4 pq[2][4][2];
#pragma unroll
        for (int mt = 0; mt < 4; ++mt) {
            f32x16 x0 = cin[0], x1 = cin[1];
#pragma unroll
            for (int t = 0; t < 8; ++t) {
                const bf16x8 a = *(const LAS bf16x8*)(lds + L_PROJ + (mt * 8 + t) * 1024 + lane * 16);
                x0 = mfma(a, asbf(qf[0][t]), x0);
                x1 = mfma(a, asbf(qf[1][t]), x1);
            }
#pragma unroll
            for (int i = 0; i < 16; ++i) { x0[i] = __builtin_amdgcn_exp2f(x0[i]); x1[i] = __builtin_amdgcn_exp2f(x1[i]); }
            pq[0][mt][0] = pack8(x0, 0); pq[0][mt][1] = pack8(x0, 1);
            pq[1][mt][0] = pack8(x1, 0); pq[1][mt][1] = pack8(x1, 1);
        }
        stamp(ws, 2, lane);
        bar();
        stamp(ws, 3, lane);
        f32x16 dn0 = {}, dn1 = {};
#pragma unroll
        for (int k = 0; k < 8; ++k) {
            const bf16x8 a = *(const LAS bf16x8*)(lds + L_KS + k * 32 + h * 16);
            dn0 = mfma(a, asbf(pq[0][k >> 1][k & 1]), dn0);
            dn1 = mfma(a, asbf(pq[1][k >> 1][k & 1]), dn1);
        }
        const float rd[2] = {__builtin_amdgcn_rcpf(dn0[0] + 1e-6f), __builtin_amdgcn_rcpf(dn1[0] + 1e-6f)};
        char* ob = gout + rowbase + (size_t)(w * 64 + r) * 256 + h * 16;
#pragma unroll
        for (int c = 0; c < 4; ++c) {
            f32x16 o[2] = {{}, {}};
#pragma unroll
            for (int k = 0; k < 8; ++k) {
                const bf16x8 a = *(const LAS bf16x8*)(lds + L_KV + ((k >> 1) * 8 + c * 2 + (k & 1)) * 1024 + lane * 16);
                o[0] = mfma(a, asbf(pq[0][k >> 1][k & 1]), o[0]);
                o[1] = mfma(a, asbf(pq[1][k >> 1][k & 1]), o[1]);
            }
#pragma unroll
            for (int u = 0; u < 2; ++u) {
#pragma unroll
                for (int g = 0; g < 4; g += 2) {
                    const unsigned ax = pack2(o[u][4 * g] * rd[u], o[u][4 * g + 1] * rd[u]), ay = pack2(o[u][4 * g + 2] * rd[u], o[u][4 * g + 3] * rd[u]);
                    const unsigned bx = pack2(o[u][4 * g + 4] * rd[u], o[u][4 * g + 5] * rd[u]), by = pack2(o[u][4 * g + 6] * rd[u], o[u][4 * g + 7] * rd[u]);
                    const auto sx = __builtin_amdgcn_permlane32_swap(ax, bx, false, false);
                    const auto sy = __builtin_amdgcn_permlane32_swap(ay, by, false, false);
                    const u32x4 v = {sx[0], sy[0], sx[1], sy[1]};
                    *(GAS u32x4*)(ob + u * 8192 + c * 64 + g * 16) = v;
                }
            }
        }
        stamp(ws, 4, lane);
        return;
    }

    char* sync = ws + WS_SYNC + (head * 4 + w) * 256;
    const unsigned long long tag = __hip_atomic_load((gu64*)(sync + 64), __ATOMIC_RELAXED, __HIP_MEMORY_SCOPE_AGENT) + 1ull;
    u32x4 pb[8];
#pragma unroll
    for (int t = 0; t < 8; ++t) pb[t] = *(const GAS u32x4*)(gproj + (32 * w + r) * 256 + 32 * t + 16 * h);
    asm volatile("" : "+v"(pb[0]), "+v"(pb[1]), "+v"(pb[2]), "+v"(pb[3]), "+v"(pb[4]), "+v"(pb[5]), "+v"(pb[6]), "+v"(pb[7]));
#pragma unroll
    for (int t = 0; t < 8; ++t)
#pragma unroll
        for (int i = 0; i < 4; ++i) pb[t][i] = pack2(bflo(pb[t][i]) * kC1, bfhi(pb[t][i]) * kC1);
    unsigned ka[8], va[2][4];
#pragma unroll
    for (int t = 0; t < 8; ++t) ka[t] = 256 * r + 16 * ((2 * t + h) ^ swz(r));
    {
        const unsigned q = (lane & 15) >> 2, p = lane & 3, blk = (lane >> 4) & 1;
#pragma unroll
        for (int t2 = 0; t2 < 2; ++t2)
#pragma unroll
            for (int c = 0; c < 4; ++c) {
                const unsigned row = 8 * t2 + 4 * h + q;
                va[t2][c] = 8192 + 256 * row + 16 * ((4 * c + 2 * blk + (p >> 1)) ^ swz(row)) + 8 * (p & 1);
            }
    }
    const u32x4 ones = {0x3f803f80u, 0u, 0u, 0u};
    f32x16 kv[4] = {{}, {}, {}, {}};
    float ks0 = 0.f, ks1 = 0.f, ks2 = 0.f, ks3 = 0.f;
#pragma unroll
    for (int st = 0; st < 8; ++st) {
        bar();
        if (st == 0) stamp(ws, 1, lane);
        if (st == 2) {
#pragma unroll
            for (int t = 0; t < 8; ++t) *(LAS u32x4*)(lds + L_PROJ + (w * 8 + t) * 1024 + lane * 16) = pb[t];
        }
        u32x4 kf[8];
#pragma unroll
        for (int t = 0; t < 8; ++t) kf[t] = *(const LAS u32x4*)(lds + st * 16384 + ka[t]);
        s16x4 vf[2][4][2];
#pragma unroll
        for (int ss = 0; ss < 2; ++ss)
#pragma unroll
            for (int c = 0; c < 4; ++c)
#pragma unroll
                for (int t2 = 0; t2 < 2; ++t2)
                    vf[ss][c][t2] = __builtin_amdgcn_ds_read_tr16_b64_v4i16((LAS s16x4*)(lds + st * 16384 + ss * 4096 + va[t2][c]));
        __builtin_amdgcn_sched_barrier(0);
        f32x16 x = {};
        float n0 = 0.f, n1 = 0.f;
#pragma unroll
        for (int t = 0; t < 8; ++t) {
            x = mfma(asbf(kf[t]), asbf(pb[t]), x);
            n0 = dot2(kf[t][0], n0); n1 = dot2(kf[t][1], n1); n0 = dot2(kf[t][2], n0); n1 = dot2(kf[t][3], n1);
        }
        const float cpart = (n0 + n1) * kCN + (h ? 0.f : kCB);
        const unsigned chi = pack2(cpart, 0.f) & 0xffffu;
        const unsigned cpk = pack2(bflo(chi), cpart - bflo(chi));
        const u32x4 an = {cpk, 0u, 0u, 0u};
        x = mfma(asbf(an), asbf(ones), x);
#pragma unroll
        for (int i = 0; i < 16; ++i) x[i] = __builtin_amdgcn_exp2f(x[i]);
#pragma unroll
        for (int ss = 0; ss < 2; ++ss) {
            const u32x4 a = pack8(x, ss);
#pragma unroll
            for (int c = 0; c < 4; ++c) {
                const s16x8 bv = __builtin_shufflevector(vf[ss][c][0], vf[ss][c][1], 0, 1, 2, 3, 4, 5, 6, 7);
                kv[c] = mfma(asbf(a), __builtin_bit_cast(bf16x8, bv), kv[c]);
            }
        }
#pragma unroll
        for (int i = 0; i < 16; i += 4) { ks0 += x[i]; ks1 += x[i + 1]; ks2 += x[i + 2]; ks3 += x[i + 3]; }
        asm volatile("" : "+v"(ks0), "+v"(ks1), "+v"(ks2), "+v"(ks3));
    }
    stamp(ws, 2, lane);
    const __amdgpu_buffer_rsrc_t wr = __builtin_amdgcn_make_buffer_rsrc(ws, 0, WS_BYTES, 0x00020000);
    const unsigned pbase = WS_P + ((head * 8 + member) * 4 + w) * 8192 + lane * 16;
#pragma unroll
    for (int c = 0; c < 4; ++c)
#pragma unroll
        for (int s = 0; s < 2; ++s) __builtin_amdgcn_raw_buffer_store_b128(pack8(kv[c], s), wr, pbase + (c * 2 + s) * 1024, 0, 16);
    const float ks = halfsum((ks0 + ks1) + (ks2 + ks3));
    if (h == 0) __builtin_amdgcn_raw_buffer_store_b32(__float_as_uint(ks), wr, WS_KS + (head * 8 + member) * 512 + (32 * w + r) * 4, 0, 16);
    asm volatile("s_waitcnt vmcnt(0)" ::: "memory");
    if (lane == 0) __hip_atomic_store((gu64*)sync + member, tag, __ATOMIC_RELAXED, __HIP_MEMORY_SCOPE_AGENT);
    stamp(ws, 3, lane);
    poll8((const gu64*)sync, tag, lane);
    stamp(ws, 4, lane);
    if (member == 0 && lane == 0) __hip_atomic_store((gu64*)(sync + 64), tag, __ATOMIC_RELAXED, __HIP_MEMORY_SCOPE_AGENT);
    u32x4 part[8];
#pragma unroll
    for (int jj = 0; jj < 8; ++jj) part[jj] = __builtin_amdgcn_raw_buffer_load_b128(wr, WS_P + ((head * 8 + jj) * 4 + w) * 8192 + member * 1024 + lane * 16, 0, 16);
    float kp[4];
#pragma unroll
    for (int i = 0; i < 4; ++i) kp[i] = __uint_as_float(__builtin_amdgcn_raw_buffer_load_b32(wr, WS_KS + (head * 8 + 4 * h + i) * 512 + (32 * w + r) * 4, 0, 16));
    u32x4 fin;
#pragma unroll
    for (int i = 0; i < 4; ++i) {
        float lo = 0.f, hi = 0.f;
#pragma unroll
        for (int jj = 0; jj < 8; ++jj) { lo += bflo(part[jj][i]); hi += bfhi(part[jj][i]); }
        fin[i] = pack2(lo, hi);
    }
    const unsigned fbase = WS_F + (head * 4 + w) * 8192 + lane * 16;
    __builtin_amdgcn_raw_buffer_store_b128(fin, wr, fbase + member * 1024, 0, 16);
    asm volatile("s_waitcnt vmcnt(0)" ::: "memory");
    if (lane == 0) __hip_atomic_store((gu64*)(sync + 128) + member, tag, __ATOMIC_RELAXED, __HIP_MEMORY_SCOPE_AGENT);
    stamp(ws, 5, lane);
    {
        const float kt = halfsum((kp[0] + kp[1]) + (kp[2] + kp[3]));
        const unsigned s2 = r >> 4, jhi = (r >> 3) & 1, hh = (r >> 2) & 1, jlo = r & 3;
        if (h == 0) *(LAS unsigned short*)(lds + L_KS + ((w * 2 + s2) * 2 + hh) * 16 + (4 * jhi + jlo) * 2) = (unsigned short)(pack2(kt, 0.f) & 0xffffu);
    }
    poll8((const gu64*)(sync + 128), tag, lane);
    stamp(ws, 6, lane);
    u32x4 ff[8];
#pragma unroll
    for (int f = 0; f < 8; ++f) ff[f] = __builtin_amdgcn_raw_buffer_load_b128(wr, fbase + f * 1024, 0, 16);
#pragma unroll
    for (int f = 0; f < 8; ++f) *(LAS u32x4*)(lds + L_KV + (w * 8 + f) * 1024 + lane * 16) = ff[f];
    stamp(ws, 7, lane);
    bar();
}

extern "C" void kernel_launch(void* const* d_in, const int* in_sizes, int n_in, void* d_out, int out_size, void* d_ws, size_t ws_size,
                              hipStream_t stream) {
    performer_kernel<<<256, 512, 0, stream>>>((const char*)d_in[0], (const char*)d_in[1], (const char*)d_in[2], (const char*)d_in[3],
                                              (char*)d_out, (char*)d_ws);
}
```

```cpp
#define STAMP 1
#define PROBE_STAT 0
#include <hip/hip_runtime.h>
#include <hip/hip_bf16.h>
#include <stdint.h>

typedef __bf16 bf16x2 __attribute__((ext_vector_type(2)));
typedef __bf16 bf16x8 __attribute__((ext_vector_type(8)));
typedef short s16x4 __attribute__((ext_vector_type(4)));
typedef short s16x8 __attribute__((ext_vector_type(8)));
typedef float f32x2 __attribute__((ext_vector_type(2)));
typedef float f32x16 __attribute__((ext_vector_type(16)));
typedef unsigned u32x2 __attribute__((ext_vector_type(2)));
typedef unsigned u32x4 __attribute__((ext_vector_type(4)));
#define LAS __attribute__((address_space(3)))
#define GAS __attribute__((address_space(1)))
#define DI __device__ __forceinline__
typedef GAS unsigned long long gu64;

#define NTL " nt"

constexpr int kS = 2048;
constexpr float kC1 = 0.29730177875068026f * 1.4426950408889634f;
constexpr float kCN = -0.5f * 0.08838834764831845f * 1.4426950408889634f;
constexpr float kCB = -3.5f;

constexpr unsigned WS_SYNC = 0;
constexpr unsigned WS_KS = 0x10000;
constexpr unsigned WS_F = 0x40000;
constexpr unsigned WS_P = 0x200000;
constexpr unsigned WS_STAMP = 0x1000000;
constexpr unsigned WS_BYTES = 0x1100000;

constexpr unsigned L_PROJ = 0;
constexpr unsigned L_KV = 32768;
constexpr unsigned L_KS = 131072;
constexpr unsigned L_BYTES = 131072 + 256;

constexpr unsigned kSpinCap = 1u << 20;

DI f32x16 mfma(bf16x8 a, bf16x8 b, f32x16 c) { return __builtin_amdgcn_mfma_f32_32x32x16_bf16(a, b, c, 0, 0, 0); }
DI unsigned pack2(float lo, float hi) { f32x2 f = {lo, hi}; return __builtin_bit_cast(unsigned, __builtin_convertvector(f, bf16x2)); }
DI float bflo(unsigned u) { return __uint_as_float(u << 16); }
DI float bfhi(unsigned u) { return __uint_as_float(u & 0xffff0000u); }
DI float dot2(unsigned u, float acc) { bf16x2 a = __builtin_bit_cast(bf16x2, u); return __builtin_amdgcn_fdot2_f32_bf16(a, a, acc, false); }
DI float sumsq(u32x4 v, float acc) { acc = dot2(v[0], acc); acc = dot2(v[1], acc); acc = dot2(v[2], acc); return dot2(v[3], acc); }
DI float halfsum(float x) { auto r = __builtin_amdgcn_permlane32_swap(__float_as_uint(x), __float_as_uint(x), false, false); return __uint_as_float(r[0]) + __uint_as_float(r[1]); }
DI bf16x8 asbf(u32x4 v) { return __builtin_bit_cast(bf16x8, v); }
DI u32x4 pack8(const f32x16& x, int s) {
    u32x4 p; p[0] = pack2(x[8 * s], x[8 * s + 1]); p[1] = pack2(x[8 * s + 2], x[8 * s + 3]); p[2] = pack2(x[8 * s + 4], x[8 * s + 5]); p[3] = pack2(x[8 * s + 6], x[8 * s + 7]); return p;
}
DI void bar() { asm volatile("s_waitcnt lgkmcnt(0)\n\ts_barrier" ::: "memory"); }
DI unsigned swz(unsigned row) { return ((row & 3) << 2) | ((row >> 2) & 3); }
DI void stampc(char* ws, int idx, int lane) {
    unsigned long long t = __builtin_amdgcn_s_memtime();
    if (lane == 0) *(GAS unsigned long long*)(ws + WS_STAMP + ((size_t)blockIdx.x * 8 + (threadIdx.x >> 6)) * 256 + idx * 8) = t;
}
DI void stamp(char* ws, int idx, int lane) {
    unsigned long long t = __builtin_amdgcn_s_memrealtime();
    if (lane == 0) *(GAS unsigned long long*)(ws + WS_STAMP + ((size_t)blockIdx.x * 8 + (threadIdx.x >> 6)) * 256 + idx * 8) = t;
}
DI void poll8(const gu64* line, unsigned long long tag, int lane) {
    for (unsigned spins = 0; spins < kSpinCap; ++spins) {
        unsigned long long x = __hip_atomic_load(line + (lane & 7), __ATOMIC_RELAXED, __HIP_MEMORY_SCOPE_AGENT);
        if (__all(x == tag)) break;
        __builtin_amdgcn_s_sleep(1);
    }
    asm volatile("" ::: "memory");
}

#define STR2(x) #x
#define STR(x) STR2(x)
#define DMA_KV(st) \
    "s_add_u32 m0, %[ldsw], " STR(st) "*16384\n\t" \
    "s_nop 0\n\t" \
    "global_load_lds_dwordx4 %[vo0], %[kb] offset:0" NTL "\n\t" \
    "global_load_lds_dwordx4 %[vo1], %[kb] offset:1024" NTL "\n\t" \
    "s_add_u32 m0, %[ldsw], " STR(st) "*16384+8192\n\t" \
    "s_nop 0\n\t" \
    "global_load_lds_dwordx4 %[vo0], %[vb] offset:0" NTL "\n\t" \
    "global_load_lds_dwordx4 %[vo1], %[vb] offset:1024" NTL "\n\t" \
    "v_add_u32 %[vo0], 0x2000, %[vo0]\n\t" \
    "v_add_u32 %[vo1], 0x2000, %[vo1]\n\t"
#define QLD(r, vq, off) "global_load_dwordx4 %[" #r "], %[" #vq "], %[qb] offset:" STR(off) NTL "\n\t"
#define QG0 QLD(q0, vq0, 0) QLD(q1, vq0, 32) QLD(q2, vq0, 64) QLD(q3, vq0, 96)
#define QG1 QLD(q4, vq0, 128) QLD(q5, vq0, 160) QLD(q6, vq0, 192) QLD(q7, vq0, 224)
#define QG2 QLD(q8, vq1, 0) QLD(q9, vq1, 32) QLD(q10, vq1, 64) QLD(q11, vq1, 96)
#define QG3 QLD(q12, vq1, 128) QLD(q13, vq1, 160) QLD(q14, vq1, 192) QLD(q15, vq1, 224)
#define WB(n) "s_waitcnt vmcnt(" STR(n) ")\n\ts_barrier\n\t"

__global__ __launch_bounds__(512, 2) void performer_kernel(const char* __restrict__ gq, const char* __restrict__ gk, const char* __restrict__ gv,
                                                           const char* __restrict__ gproj, char* __restrict__ gout, char* __restrict__ ws) {
    __shared__ __attribute__((aligned(16))) char lds[L_BYTES];
    const int lane = threadIdx.x & 63, r = lane & 31, h = lane >> 5;
    const int wv = __builtin_amdgcn_readfirstlane(threadIdx.x >> 6), w = wv & 3;
    const int b = blockIdx.x, head = (b & 7) * 4 + (b >> 6), member = (b >> 3) & 7;
    const size_t rowbase = ((size_t)head * kS + member * 256) * 256;
    stamp(ws, 0, lane);
    stampc(ws, 20, lane);

    if (wv >= 4) {
        const unsigned ldsbase = (unsigned)(size_t)(LAS char*)lds;
        const unsigned ldsw = __builtin_amdgcn_readfirstlane(ldsbase + w * 2048);
        const unsigned lr = lane >> 4, sl = lane & 15;
        unsigned vo0 = w * 2048 + lr * 256 + 16 * (sl ^ ((lr << 2) | ((2 * w) & 3)));
        unsigned vo1 = w * 2048 + lr * 256 + 16 * (sl ^ ((lr << 2) | ((2 * w + 1) & 3)));
        const unsigned vq0 = w * 16384 + r * 256 + h * 16, vq1 = vq0 + 8192;
        const char* kb = gk + rowbase; const char* vb = gv + rowbase; const char* qb = gq + rowbase;
        u32x4 q0, q1, q2, q3, q4, q5, q6, q7, q8, q9, q10, q11, q12, q13, q14, q15; unsigned keep;
        asm volatile("s_mov_b32 %[keep], m0\n\t"
                     DMA_KV(0) DMA_KV(1) DMA_KV(2) DMA_KV(3) DMA_KV(4) DMA_KV(5)
                     WB(20) DMA_KV(6) WB(20) DMA_KV(7) WB(20) QG0 WB(20) QG1 WB(20) QG2 WB(20) QG3 WB(20) WB(16)
                     "s_waitcnt vmcnt(0)\n\ts_mov_b32 m0, %[keep]"
                     : [keep] "=&s"(keep), [vo0] "+v"(vo0), [vo1] "+v"(vo1),
                       [q0] "=&v"(q0), [q1] "=&v"(q1), [q2] "=&v"(q2), [q3] "=&v"(q3), [q4] "=&v"(q4), [q5] "=&v"(q5), [q6] "=&v"(q6), [q7] "=&v"(q7),
                       [q8] "=&v"(q8), [q9] "=&v"(q9), [q10] "=&v"(q10), [q11] "=&v"(q11), [q12] "=&v"(q12), [q13] "=&v"(q13), [q14] "=&v"(q14), [q15] "=&v"(q15)
                     : [ldsw] "s"(ldsw), [kb] "s"(kb), [vb] "s"(vb), [qb] "s"(qb), [vq0] "v"(vq0), [vq1] "v"(vq1)
                     : "memory", "scc");
        stamp(ws, 1, lane);
        const u32x4 qf[2][8] = {{q0, q1, q2, q3, q4, q5, q6, q7}, {q8, q9, q10, q11, q12, q13, q14, q15}};
        f32x16 cin[2];
#pragma unroll
        for (int u = 0; u < 2; ++u) {
            float n = 0.f;
#pragma unroll
            for (int t = 0; t < 8; ++t) n = sumsq(qf[u][t], n);
            const float c = halfsum(n) * kCN + kCB;
#pragma unroll
            for (int i = 0; i < 16; ++i) cin[u][i] = c;
        }
        u32x4 pq[2][4][2];
#pragma unroll
        for (int mt = 0; mt < 4; ++mt) {
            f32x16 x0 = cin[0], x1 = cin[1];
#pragma unroll
            for (int t = 0; t < 8; ++t) {
                const bf16x8 a = *(const LAS bf16x8*)(lds + L_PROJ + (mt * 8 + t) * 1024 + lane * 16);
                x0 = mfma(a, asbf(qf[0][t]), x0);
                x1 = mfma(a, asbf(qf[1][t]), x1);
            }
#pragma unroll
            for (int i = 0; i < 16; ++i) { x0[i] = __builtin_amdgcn_exp2f(x0[i]); x1[i] = __builtin_amdgcn_exp2f(x1[i]); }
            pq[0][mt][0] = pack8(x0, 0); pq[0][mt][1] = pack8(x0, 1);
            pq[1][mt][0] = pack8(x1, 0); pq[1][mt][1] = pack8(x1, 1);
        }
        stamp(ws, 2, lane);
        bar();
        stamp(ws, 3, lane);
        f32x16 dn0 = {}, dn1 = {};
#pragma unroll
        for (int k = 0; k < 8; ++k) {
            const bf16x8 a = *(const LAS bf16x8*)(lds + L_KS + k * 32 + h * 16);
            dn0 = mfma(a, asbf(pq[0][k >> 1][k & 1]), dn0);
            dn1 = mfma(a, asbf(pq[1][k >> 1][k & 1]), dn1);
        }
        const float rd[2] = {__builtin_amdgcn_rcpf(dn0[0] + 1e-6f), __builtin_amdgcn_rcpf(dn1[0] + 1e-6f)};
        char* ob = gout + rowbase + (size_t)(w * 64 + r) * 256 + h * 16;
#pragma unroll
        for (int c = 0; c < 4; ++c) {
            f32x16 o[2] = {{}, {}};
#pragma unroll
            for (int k = 0; k < 8; ++k) {
                const bf16x8 a = *(const LAS bf16x8*)(lds + L_KV + ((k >> 1) * 8 + c * 2 + (k & 1)) * 1024 + lane * 16);
                o[0] = mfma(a, asbf(pq[0][k >> 1][k & 1]), o[0]);
                o[1] = mfma(a, asbf(pq[1][k >> 1][k & 1]), o[1]);
            }
#pragma unroll
            for (int u = 0; u < 2; ++u) {
#pragma unroll
                for (int g = 0; g < 4; g += 2) {
                    const unsigned ax = pack2(o[u][4 * g] * rd[u], o[u][4 * g + 1] * rd[u]), ay = pack2(o[u][4 * g + 2] * rd[u], o[u][4 * g + 3] * rd[u]);
                    const unsigned bx = pack2(o[u][4 * g + 4] * rd[u], o[u][4 * g + 5] * rd[u]), by = pack2(o[u][4 * g + 6] * rd[u], o[u][4 * g + 7] * rd[u]);
                    const auto sx = __builtin_amdgcn_permlane32_swap(ax, bx, false, false);
                    const auto sy = __builtin_amdgcn_permlane32_swap(ay, by, false, false);
                    const u32x4 v = {sx[0], sy[0], sx[1], sy[1]};
                    *(GAS u32x4*)(ob + u * 8192 + c * 64 + g * 16) = v;
                }
            }
        }
        stamp(ws, 4, lane);
        stampc(ws, 21, lane);
        return;
    }

    char* sync = ws + WS_SYNC + (head * 4 + w) * 256;
    const unsigned long long tag = __hip_atomic_load((gu64*)(sync + 64), __ATOMIC_RELAXED, __HIP_MEMORY_SCOPE_AGENT) + 1ull;
    u32x4 pb[8];
#pragma unroll
    for (int t = 0; t < 8; ++t) pb[t] = *(const GAS u32x4*)(gproj + (32 * w + r) * 256 + 32 * t + 16 * h);
    asm volatile("" : "+v"(pb[0]), "+v"(pb[1]), "+v"(pb[2]), "+v"(pb[3]), "+v"(pb[4]), "+v"(pb[5]), "+v"(pb[6]), "+v"(pb[7]));
#pragma unroll
    for (int t = 0; t < 8; ++t)
#pragma unroll
        for (int i = 0; i < 4; ++i) pb[t][i] = pack2(bflo(pb[t][i]) * kC1, bfhi(pb[t][i]) * kC1);
    unsigned ka[8], va[2][4];
#pragma unroll
    for (int t = 0; t < 8; ++t) ka[t] = 256 * r + 16 * ((2 * t + h) ^ swz(r));
    {
        const unsigned q = (lane & 15) >> 2, p = lane & 3, blk = (lane >> 4) & 1;
#pragma unroll
        for (int t2 = 0; t2 < 2; ++t2)
#pragma unroll
            for (int c = 0; c < 4; ++c) {
                const unsigned row = 8 * t2 + 4 * h + q;
                va[t2][c] = 8192 + 256 * row + 16 * ((4 * c + 2 * blk + (p >> 1)) ^ swz(row)) + 8 * (p & 1);
            }
    }
    const u32x4 ones = {0x3f803f80u, 0u, 0u, 0u};
    f32x16 kv[4] = {{}, {}, {}, {}};
    float ks0 = 0.f, ks1 = 0.f, ks2 = 0.f, ks3 = 0.f;
#pragma unroll
    for (int st = 0; st < 8; ++st) {
        bar();
        if (st == 0) stamp(ws, 1, lane);
        stamp(ws, 8 + st, lane);
        if (st == 2) {
#pragma unroll
            for (int t = 0; t < 8; ++t) *(LAS u32x4*)(lds + L_PROJ + (w * 8 + t) * 1024 + lane * 16) = pb[t];
        }
        u32x4 kf[8];
#pragma unroll
        for (int t = 0; t < 8; ++t) kf[t] = *(const LAS u32x4*)(lds + st * 16384 + ka[t]);
        s16x4 vf[2][4][2];
#pragma unroll
        for (int ss = 0; ss < 2; ++ss)
#pragma unroll
            for (int c = 0; c < 4; ++c)
#pragma unroll
                for (int t2 = 0; t2 < 2; ++t2)
                    vf[ss][c][t2] = __builtin_amdgcn_ds_read_tr16_b64_v4i16((LAS s16x4*)(lds + st * 16384 + ss * 4096 + va[t2][c]));
        __builtin_amdgcn_sched_barrier(0);
        f32x16 x = {};
        float n0 = 0.f, n1 = 0.f;
#pragma unroll
        for (int t = 0; t < 8; ++t) {
            x = mfma(asbf(kf[t]), asbf(pb[t]), x);
            n0 = dot2(kf[t][0], n0); n1 = dot2(kf[t][1], n1); n0 = dot2(kf[t][2], n0); n1 = dot2(kf[t][3], n1);
        }
        const float cpart = (n0 + n1) * kCN + (h ? 0.f : kCB);
        const unsigned chi = pack2(cpart, 0.f) & 0xffffu;
        const unsigned cpk = pack2(bflo(chi), cpart - bflo(chi));
        const u32x4 an = {cpk, 0u, 0u, 0u};
        x = mfma(asbf(an), asbf(ones), x);
#pragma unroll
        for (int i = 0; i < 16; ++i) x[i] = __builtin_amdgcn_exp2f(x[i]);
#pragma unroll
        for (int ss = 0; ss < 2; ++ss) {
            const u32x4 a = pack8(x, ss);
#pragma unroll
            for (int c = 0; c < 4; ++c) {
                const s16x8 bv = __builtin_shufflevector(vf[ss][c][0], vf[ss][c][1], 0, 1, 2, 3, 4, 5, 6, 7);
                kv[c] = mfma(asbf(a), __builtin_bit_cast(bf16x8, bv), kv[c]);
            }
        }
#pragma unroll
        for (int i = 0; i < 16; i += 4) { ks0 += x[i]; ks1 += x[i + 1]; ks2 += x[i + 2]; ks3 += x[i + 3]; }
        asm volatile("" : "+v"(ks0), "+v"(ks1), "+v"(ks2), "+v"(ks3));
    }
    stamp(ws, 2, lane);
    const __amdgpu_buffer_rsrc_t wr = __builtin_amdgcn_make_buffer_rsrc(ws, 0, WS_BYTES, 0x00020000);
    const unsigned pbase = WS_P + ((head * 8 + member) * 4 + w) * 8192 + lane * 16;
#pragma unroll
    for (int c = 0; c < 4; ++c)
#pragma unroll
        for (int s = 0; s < 2; ++s) __builtin_amdgcn_raw_buffer_store_b128(pack8(kv[c], s), wr, pbase + (c * 2 + s) * 1024, 0, 16);
    const float ks = halfsum((ks0 + ks1) + (ks2 + ks3));
    if (h == 0) __builtin_amdgcn_raw_buffer_store_b32(__float_as_uint(ks), wr, WS_KS + (head * 8 + member) * 512 + (32 * w + r) * 4, 0, 16);
    asm volatile("s_waitcnt vmcnt(0)" ::: "memory");
    if (lane == 0) __hip_atomic_store((gu64*)sync + member, tag, __ATOMIC_RELAXED, __HIP_MEMORY_SCOPE_AGENT);
    stamp(ws, 3, lane);
    poll8((const gu64*)sync, tag, lane);
    stamp(ws, 4, lane);
    if (member == 0 && lane == 0) __hip_atomic_store((gu64*)(sync + 64), tag, __ATOMIC_RELAXED, __HIP_MEMORY_SCOPE_AGENT);
    u32x4 part[8];
#pragma unroll
    for (int jj = 0; jj < 8; ++jj) part[jj] = __builtin_amdgcn_raw_buffer_load_b128(wr, WS_P + ((head * 8 + jj) * 4 + w) * 8192 + member * 1024 + lane * 16, 0, 16);
    float kp[4];
#pragma unroll
    for (int i = 0; i < 4; ++i) kp[i] = __uint_as_float(__builtin_amdgcn_raw_buffer_load_b32(wr, WS_KS + (head * 8 + 4 * h + i) * 512 + (32 * w + r) * 4, 0, 16));
    u32x4 fin;
#pragma unroll
    for (int i = 0; i < 4; ++i) {
        float lo = 0.f, hi = 0.f;
#pragma unroll
        for (int jj = 0; jj < 8; ++jj) { lo += bflo(part[jj][i]); hi += bfhi(part[jj][i]); }
        fin[i] = pack2(lo, hi);
    }
    const unsigned fbase = WS_F + (head * 4 + w) * 8192 + lane * 16;
    __builtin_amdgcn_raw_buffer_store_b128(fin, wr, fbase + member * 1024, 0, 16);
    asm volatile("s_waitcnt vmcnt(0)" ::: "memory");
    if (lane == 0) __hip_atomic_store((gu64*)(sync + 128) + member, tag, __ATOMIC_RELAXED, __HIP_MEMORY_SCOPE_AGENT);
    stamp(ws, 5, lane);
    {
        const float kt = halfsum((kp[0] + kp[1]) + (kp[2] + kp[3]));
        const unsigned s2 = r >> 4, jhi = (r >> 3) & 1, hh = (r >> 2) & 1, jlo = r & 3;
        if (h == 0) *(LAS unsigned short*)(lds + L_KS + ((w * 2 + s2) * 2 + hh) * 16 + (4 * jhi + jlo) * 2) = (unsigned short)(pack2(kt, 0.f) & 0xffffu);
    }
    poll8((const gu64*)(sync + 128), tag, lane);
    stamp(ws, 6, lane);
    u32x4 ff[8];
#pragma unroll
    for (int f = 0; f < 8; ++f) ff[f] = __builtin_amdgcn_raw_buffer_load_b128(wr, fbase + f * 1024, 0, 16);
#pragma unroll
    for (int f = 0; f < 8; ++f) *(LAS u32x4*)(lds + L_KV + (w * 8 + f) * 1024 + lane * 16) = ff[f];
    stamp(ws, 7, lane);
    bar();
}

__global__ __launch_bounds__(256) void k_probe(char* ws) {
    __shared__ unsigned long long red[2][256];
    unsigned long long t0 = ~0ull, x = 0;
    const int b = threadIdx.x;
    for (int w = 0; w < 8; ++w) {
        const unsigned long long* p = (const unsigned long long*)(ws + WS_STAMP + ((size_t)b * 8 + w) * 256);
        t0 = p[0] < t0 ? p[0] : t0;
        unsigned long long y = 0;
        if (PROBE_STAT == 1 && w >= 4) y = p[4];
        if (PROBE_STAT == 2) y = p[0];
        if (PROBE_STAT == 3 && w < 4) y = p[1];
        if (PROBE_STAT == 4 && w < 4) y = p[2];
        if (PROBE_STAT == 5 && w < 4) y = p[7];
        if (PROBE_STAT == 6 && w < 4) y = p[3];
        x = y > x ? y : x;
    }
    red[0][b] = t0; red[1][b] = x;
    __syncthreads();
    if (b == 0) {
        for (int i = 1; i < 256; ++i) { t0 = red[0][i] < t0 ? red[0][i] : t0; x = red[1][i] > x ? red[1][i] : x; }
        unsigned long long d = PROBE_STAT == 0 ? 0 : (x - t0) * 10;
        if (d > 100000) d = 100000;
        unsigned long long s0 = __builtin_amdgcn_s_memrealtime();
        while (__builtin_amdgcn_s_memrealtime() - s0 < d) __builtin_amdgcn_s_sleep(2);
    }
}
extern "C" void kernel_launch(void* const* d_in, const int* in_sizes, int n_in, void* d_out, int out_size, void* d_ws, size_t ws_size,
                              hipStream_t stream) {
    performer_kernel<<<256, 512, 0, stream>>>((const char*)d_in[0], (const char*)d_in[1], (const char*)d_in[2], (const char*)d_in[3],
                                              (char*)d_out, (char*)d_ws);
    k_probe<<<1, 256, 0, stream>>>((char*)d_ws);
}
```

```cpp
#define OAUX 0
#include <hip/hip_runtime.h>
#include <hip/hip_bf16.h>
#include <stdint.h>

typedef __bf16 bf16x2 __attribute__((ext_vector_type(2)));
typedef __bf16 bf16x8 __attribute__((ext_vector_type(8)));
typedef short s16x4 __attribute__((ext_vector_type(4)));
typedef short s16x8 __attribute__((ext_vector_type(8)));
typedef float f32x2 __attribute__((ext_vector_type(2)));
typedef float f32x16 __attribute__((ext_vector_type(16)));
typedef unsigned u32x2 __attribute__((ext_vector_type(2)));
typedef unsigned u32x4 __attribute__((ext_vector_type(4)));
#define LAS __attribute__((address_space(3)))
#define GAS __attribute__((address_space(1)))
#define DI __device__ __forceinline__
typedef GAS unsigned long long gu64;

#define NTL " nt"
#define STAMP 0

constexpr int kS = 2048;
constexpr float kC1 = 0.29730177875068026f * 1.4426950408889634f;
constexpr float kCN = -0.5f * 0.08838834764831845f * 1.4426950408889634f;
constexpr float kCB = -3.5f;

constexpr unsigned WS_SYNC = 0;
constexpr unsigned WS_KS = 0x10000;
constexpr unsigned WS_F = 0x40000;
constexpr unsigned WS_P = 0x200000;
constexpr unsigned WS_STAMP = 0x1000000;
constexpr unsigned WS_BYTES = 0x1100000;

constexpr unsigned L_PROJ = 0;
constexpr unsigned L_KV = 32768;
constexpr unsigned L_KS = 131072;
constexpr unsigned L_BYTES = 131072 + 256;

constexpr unsigned kSpinCap = 1u << 20;

DI f32x16 mfma(bf16x8 a, bf16x8 b, f32x16 c) { return __builtin_amdgcn_mfma_f32_32x32x16_bf16(a, b, c, 0, 0, 0); }
DI unsigned pack2(float lo, float hi) { f32x2 f = {lo, hi}; return __builtin_bit_cast(unsigned, __builtin_convertvector(f, bf16x2)); }
DI float bflo(unsigned u) { return __uint_as_float(u << 16); }
DI float bfhi(unsigned u) { return __uint_as_float(u & 0xffff0000u); }
DI float dot2(unsigned u, float acc) { bf16x2 a = __builtin_bit_cast(bf16x2, u); return __builtin_amdgcn_fdot2_f32_bf16(a, a, acc, false); }
DI float sumsq(u32x4 v, float acc) { acc = dot2(v[0], acc); acc = dot2(v[1], acc); acc = dot2(v[2], acc); return dot2(v[3], acc); }
DI float halfsum(float x) { auto r = __builtin_amdgcn_permlane32_swap(__float_as_uint(x), __float_as_uint(x), false, false); return __uint_as_float(r[0]) + __uint_as_float(r[1]); }
DI bf16x8 asbf(u32x4 v) { return __builtin_bit_cast(bf16x8, v); }
DI u32x4 pack8(const f32x16& x, int s) {
    u32x4 p; p[0] = pack2(x[8 * s], x[8 * s + 1]); p[1] = pack2(x[8 * s + 2], x[8 * s + 3]); p[2] = pack2(x[8 * s + 4], x[8 * s + 5]); p[3] = pack2(x[8 * s + 6], x[8 * s + 7]); return p;
}
DI void bar() { asm volatile("s_waitcnt lgkmcnt(0)\n\ts_barrier" ::: "memory"); }
DI unsigned swz(unsigned row) { return ((row & 3) << 2) | ((row >> 2) & 3); }
DI void stampc(char* ws, int idx, int lane) {
}
DI void stamp(char* ws, int idx, int lane) {
}
DI void poll8(const gu64* line, unsigned long long tag, int lane) {
    for (unsigned spins = 0; spins < kSpinCap; ++spins) {
        unsigned long long x = __hip_atomic_load(line + (lane & 7), __ATOMIC_RELAXED, __HIP_MEMORY_SCOPE_AGENT);
        if (__all(x == tag)) break;
        __builtin_amdgcn_s_sleep(1);
    }
    asm volatile("" ::: "memory");
}

#define STR2(x) #x
#define STR(x) STR2(x)
#define DMA_KV(st) \
    "s_add_u32 m0, %[ldsw], " STR(st) "*16384\n\t" \
    "s_nop 0\n\t" \
    "global_load_lds_dwordx4 %[vo0], %[kb] offset:0" NTL "\n\t" \
    "global_load_lds_dwordx4 %[vo1], %[kb] offset:1024" NTL "\n\t" \
    "s_add_u32 m0, %[ldsw], " STR(st) "*16384+8192\n\t" \
    "s_nop 0\n\t" \
    "global_load_lds_dwordx4 %[vo0], %[vb] offset:0" NTL "\n\t" \
    "global_load_lds_dwordx4 %[vo1], %[vb] offset:1024" NTL "\n\t" \
    "v_add_u32 %[vo0], 0x2000, %[vo0]\n\t" \
    "v_add_u32 %[vo1], 0x2000, %[vo1]\n\t"
#define QLD(r, vq, off) "global_load_dwordx4 %[" #r "], %[" #vq "], %[qb] offset:" STR(off) NTL "\n\t"
#define QG0 QLD(q0, vq0, 0) QLD(q1, vq0, 32) QLD(q2, vq0, 64) QLD(q3, vq0, 96)
#define QG1 QLD(q4, vq0, 128) QLD(q5, vq0, 160) QLD(q6, vq0, 192) QLD(q7, vq0, 224)
#define QG2 QLD(q8, vq1, 0) QLD(q9, vq1, 32) QLD(q10, vq1, 64) QLD(q11, vq1, 96)
#define QG3 QLD(q12, vq1, 128) QLD(q13, vq1, 160) QLD(q14, vq1, 192) QLD(q15, vq1, 224)
#define WB(n) "s_waitcnt vmcnt(" STR(n) ")\n\ts_barrier\n\t"

__global__ __launch_bounds__(512, 2) void performer_kernel(const char* __restrict__ gq, const char* __restrict__ gk, const char* __restrict__ gv,
                                                           const char* __restrict__ gproj, char* __restrict__ gout, char* __restrict__ ws) {
    __shared__ __attribute__((aligned(16))) char lds[L_BYTES];
    const int lane = threadIdx.x & 63, r = lane & 31, h = lane >> 5;
    const int wv = __builtin_amdgcn_readfirstlane(threadIdx.x >> 6), w = wv & 3;
    const int b = blockIdx.x, head = (b & 7) * 4 + (b >> 6), member = (b >> 3) & 7;
    const size_t rowbase = ((size_t)head * kS + member * 256) * 256;
    stamp(ws, 0, lane);
    stampc(ws, 20, lane);

    if (wv >= 4) {
        const unsigned ldsbase = (unsigned)(size_t)(LAS char*)lds;
        const unsigned ldsw = __builtin_amdgcn_readfirstlane(ldsbase + w * 2048);
        const unsigned lr = lane >> 4, sl = lane & 15;
        unsigned vo0 = w * 2048 + lr * 256 + 16 * (sl ^ ((lr << 2) | ((2 * w) & 3)));
        unsigned vo1 = w * 2048 + lr * 256 + 16 * (sl ^ ((lr << 2) | ((2 * w + 1) & 3)));
        const unsigned vq0 = w * 16384 + r * 256 + h * 16, vq1 = vq0 + 8192;
        const char* kb = gk + rowbase; const char* vb = gv + rowbase; const char* qb = gq + rowbase;
        u32x4 q0, q1, q2, q3, q4, q5, q6, q7, q8, q9, q10, q11, q12, q13, q14, q15; unsigned keep;
        asm volatile("s_mov_b32 %[keep], m0\n\t"
                     DMA_KV(0) DMA_KV(1) DMA_KV(2) DMA_KV(3) DMA_KV(4) DMA_KV(5)
                     WB(20) DMA_KV(6) WB(20) DMA_KV(7) WB(20) QG0 WB(20) QG1 WB(20) QG2 WB(20) QG3 WB(20) WB(16)
                     "s_waitcnt vmcnt(0)\n\ts_mov_b32 m0, %[keep]"
                     : [keep] "=&s"(keep), [vo0] "+v"(vo0), [vo1] "+v"(vo1),
                       [q0] "=&v"(q0), [q1] "=&v"(q1), [q2] "=&v"(q2), [q3] "=&v"(q3), [q4] "=&v"(q4), [q5] "=&v"(q5), [q6] "=&v"(q6), [q7] "=&v"(q7),
                       [q8] "=&v"(q8), [q9] "=&v"(q9), [q10] "=&v"(q10), [q11] "=&v"(q11), [q12] "=&v"(q12), [q13] "=&v"(q13), [q14] "=&v"(q14), [q15] "=&v"(q15)
                     : [ldsw] "s"(ldsw), [kb] "s"(kb), [vb] "s"(vb), [qb] "s"(qb), [vq0] "v"(vq0), [vq1] "v"(vq1)
                     : "memory", "scc");
        stamp(ws, 1, lane);
        const u32x4 qf[2][8] = {{q0, q1, q2, q3, q4, q5, q6, q7}, {q8, q9, q10, q11, q12, q13, q14, q15}};
        f32x16 cin[2];
#pragma unroll
        for (int u = 0; u < 2; ++u) {
            float n = 0.f;
#pragma unroll
            for (int t = 0; t < 8; ++t) n = sumsq(qf[u][t], n);
            const float c = halfsum(n) * kCN + kCB;
#pragma unroll
            for (int i = 0; i < 16; ++i) cin[u][i] = c;
        }
        u32x4 pq[2][4][2];
#pragma unroll
        for (int mt = 0; mt < 4; ++mt) {
            f32x16 x0 = cin[0], x1 = cin[1];
#pragma unroll
            for (int t = 0; t < 8; ++t) {
                const bf16x8 a = *(const LAS bf16x8*)(lds + L_PROJ + (mt * 8 + t) * 1024 + lane * 16);
                x0 = mfma(a, asbf(qf[0][t]), x0);
                x1 = mfma(a, asbf(qf[1][t]), x1);
            }
#pragma unroll
            for (int i = 0; i < 16; ++i) { x0[i] = __builtin_amdgcn_exp2f(x0[i]); x1[i] = __builtin_amdgcn_exp2f(x1[i]); }
            pq[0][mt][0] = pack8(x0, 0); pq[0][mt][1] = pack8(x0, 1);
            pq[1][mt][0] = pack8(x1, 0); pq[1][mt][1] = pack8(x1, 1);
        }
        stamp(ws, 2, lane);
        bar();
        stamp(ws, 3, lane);
        f32x16 dn0 = {}, dn1 = {};
#pragma unroll
        for (int k = 0; k < 8; ++k) {
            const bf16x8 a = *(const LAS bf16x8*)(lds + L_KS + k * 32 + h * 16);
            dn0 = mfma(a, asbf(pq[0][k >> 1][k & 1]), dn0);
            dn1 = mfma(a, asbf(pq[1][k >> 1][k & 1]), dn1);
        }
        const float rd[2] = {__builtin_amdgcn_rcpf(dn0[0] + 1e-6f), __builtin_amdgcn_rcpf(dn1[0] + 1e-6f)};
        char* ob = gout + rowbase + (size_t)(w * 64 + r) * 256 + h * 16;
        const __amdgpu_buffer_rsrc_t orsrc = __builtin_amdgcn_make_buffer_rsrc(gout + rowbase, 0, 65536, 0x00020000);
        const unsigned obo = (w * 64 + r) * 256 + h * 16;
#pragma unroll
        for (int c = 0; c < 4; ++c) {
            f32x16 o[2] = {{}, {}};
#pragma unroll
            for (int k = 0; k < 8; ++k) {
                const bf16x8 a = *(const LAS bf16x8*)(lds + L_KV + ((k >> 1) * 8 + c * 2 + (k & 1)) * 1024 + lane * 16);
                o[0] = mfma(a, asbf(pq[0][k >> 1][k & 1]), o[0]);
                o[1] = mfma(a, asbf(pq[1][k >> 1][k & 1]), o[1]);
            }
#pragma unroll
            for (int u = 0; u < 2; ++u) {
#pragma unroll
                for (int g = 0; g < 4; g += 2) {
                    const unsigned ax = pack2(o[u][4 * g] * rd[u], o[u][4 * g + 1] * rd[u]), ay = pack2(o[u][4 * g + 2] * rd[u], o[u][4 * g + 3] * rd[u]);
                    const unsigned bx = pack2(o[u][4 * g + 4] * rd[u], o[u][4 * g + 5] * rd[u]), by = pack2(o[u][4 * g + 6] * rd[u], o[u][4 * g + 7] * rd[u]);
                    const auto sx = __builtin_amdgcn_permlane32_swap(ax, bx, false, false);
                    const auto sy = __builtin_amdgcn_permlane32_swap(ay, by, false, false);
                    const u32x4 v = {sx[0], sy[0], sx[1], sy[1]};
                    __builtin_amdgcn_raw_buffer_store_b128(v, orsrc, obo + u * 8192 + c * 64 + g * 16, 0, OAUX);
                }
            }
        }
        stamp(ws, 4, lane);
        stampc(ws, 21, lane);
        return;
    }

    char* sync = ws + WS_SYNC + (head * 4 + w) * 256;
    const unsigned long long tag = __hip_atomic_load((gu64*)(sync + 64), __ATOMIC_RELAXED, __HIP_MEMORY_SCOPE_AGENT) + 1ull;
    u32x4 pb[8];
#pragma unroll
    for (int t = 0; t < 8; ++t) pb[t] = *(const GAS u32x4*)(gproj + (32 * w + r) * 256 + 32 * t + 16 * h);
    asm volatile("" : "+v"(pb[0]), "+v"(pb[1]), "+v"(pb[2]), "+v"(pb[3]), "+v"(pb[4]), "+v"(pb[5]), "+v"(pb[6]), "+v"(pb[7]));
#pragma unroll
    for (int t = 0; t < 8; ++t)
#pragma unroll
        for (int i = 0; i < 4; ++i) pb[t][i] = pack2(bflo(pb[t][i]) * kC1, bfhi(pb[t][i]) * kC1);
    unsigned ka[8], va[2][4];
#pragma unroll
    for (int t = 0; t < 8; ++t) ka[t] = 256 * r + 16 * ((2 * t + h) ^ swz(r));
    {
        const unsigned q = (lane & 15) >> 2, p = lane & 3, blk = (lane >> 4) & 1;
#pragma unroll
        for (int t2 = 0; t2 < 2; ++t2)
#pragma unroll
            for (int c = 0; c < 4; ++c) {
                const unsigned row = 8 * t2 + 4 * h + q;
                va[t2][c] = 8192 + 256 * row + 16 * ((4 * c + 2 * blk + (p >> 1)) ^ swz(row)) + 8 * (p & 1);
            }
    }
    const u32x4 ones = {0x3f803f80u, 0u, 0u, 0u};
    f32x16 kv[4] = {{}, {}, {}, {}};
    float ks0 = 0.f, ks1 = 0.f, ks2 = 0.f, ks3 = 0.f;
#pragma unroll
    for (int st = 0; st < 8; ++st) {
        bar();
        if (st == 0) stamp(ws, 1, lane);
        stamp(ws, 8 + st, lane);
        if (st == 2) {
#pragma unroll
            for (int t = 0; t < 8; ++t) *(LAS u32x4*)(lds + L_PROJ + (w * 8 + t) * 1024 + lane * 16) = pb[t];
        }
        u32x4 kf[8];
#pragma unroll
        for (int t = 0; t < 8; ++t) kf[t] = *(const LAS u32x4*)(lds + st * 16384 + ka[t]);
        s16x4 vf[2][4][2];
#pragma unroll
        for (int ss = 0; ss < 2; ++ss)
#pragma unroll
            for (int c = 0; c < 4; ++c)
#pragma unroll
                for (int t2 = 0; t2 < 2; ++t2)
                    vf[ss][c][t2] = __builtin_amdgcn_ds_read_tr16_b64_v4i16((LAS s16x4*)(lds + st * 16384 + ss * 4096 + va[t2][c]));
        __builtin_amdgcn_sched_barrier(0);
        f32x16 x = {};
        float n0 = 0.f, n1 = 0.f;
#pragma unroll
        for (int t = 0; t < 8; ++t) {
            x = mfma(asbf(kf[t]), asbf(pb[t]), x);
            n0 = dot2(kf[t][0], n0); n1 = dot2(kf[t][1], n1); n0 = dot2(kf[t][2], n0); n1 = dot2(kf[t][3], n1);
        }
        const float cpart = (n0 + n1) * kCN + (h ? 0.f : kCB);
        const unsigned chi = pack2(cpart, 0.f) & 0xffffu;
        const unsigned cpk = pack2(bflo(chi), cpart - bflo(chi));
        const u32x4 an = {cpk, 0u, 0u, 0u};
        x = mfma(asbf(an), asbf(ones), x);
#pragma unroll
        for (int i = 0; i < 16; ++i) x[i] = __builtin_amdgcn_exp2f(x[i]);
#pragma unroll
        for (int ss = 0; ss < 2; ++ss) {
            const u32x4 a = pack8(x, ss);
#pragma unroll
            for (int c = 0; c < 4; ++c) {
                const s16x8 bv = __builtin_shufflevector(vf[ss][c][0], vf[ss][c][1], 0, 1, 2, 3, 4, 5, 6, 7);
                kv[c] = mfma(asbf(a), __builtin_bit_cast(bf16x8, bv), kv[c]);
            }
        }
#pragma unroll
        for (int i = 0; i < 16; i += 4) { ks0 += x[i]; ks1 += x[i + 1]; ks2 += x[i + 2]; ks3 += x[i + 3]; }
        asm volatile("" : "+v"(ks0), "+v"(ks1), "+v"(ks2), "+v"(ks3));
    }
    stamp(ws, 2, lane);
    const __amdgpu_buffer_rsrc_t wr = __builtin_amdgcn_make_buffer_rsrc(ws, 0, WS_BYTES, 0x00020000);
    const unsigned pbase = WS_P + ((head * 8 + member) * 4 + w) * 8192 + lane * 16;
#pragma unroll
    for (int c = 0; c < 4; ++c)
#pragma unroll
        for (int s = 0; s < 2; ++s) __builtin_amdgcn_raw_buffer_store_b128(pack8(kv[c], s), wr, pbase + (c * 2 + s) * 1024, 0, 16);
    const float ks = halfsum((ks0 + ks1) + (ks2 + ks3));
    if (h == 0) __builtin_amdgcn_raw_buffer_store_b32(__float_as_uint(ks), wr, WS_KS + (head * 8 + member) * 512 + (32 * w + r) * 4, 0, 16);
    asm volatile("s_waitcnt vmcnt(0)" ::: "memory");
    if (lane == 0) __hip_atomic_store((gu64*)sync + member, tag, __ATOMIC_RELAXED, __HIP_MEMORY_SCOPE_AGENT);
    stamp(ws, 3, lane);
    poll8((const gu64*)sync, tag, lane);
    stamp(ws, 4, lane);
    if (member == 0 && lane == 0) __hip_atomic_store((gu64*)(sync + 64), tag, __ATOMIC_RELAXED, __HIP_MEMORY_SCOPE_AGENT);
    u32x4 part[8];
#pragma unroll
    for (int jj = 0; jj < 8; ++jj) part[jj] = __builtin_amdgcn_raw_buffer_load_b128(wr, WS_P + ((head * 8 + jj) * 4 + w) * 8192 + member * 1024 + lane * 16, 0, 16);
    float kp[4];
#pragma unroll
    for (int i = 0; i < 4; ++i) kp[i] = __uint_as_float(__builtin_amdgcn_raw_buffer_load_b32(wr, WS_KS + (head * 8 + 4 * h + i) * 512 + (32 * w + r) * 4, 0, 16));
    u32x4 fin;
#pragma unroll
    for (int i = 0; i < 4; ++i) {
        float lo = 0.f, hi = 0.f;
#pragma unroll
        for (int jj = 0; jj < 8; ++jj) { lo += bflo(part[jj][i]); hi += bfhi(part[jj][i]); }
        fin[i] = pack2(lo, hi);
    }
    const unsigned fbase = WS_F + (head * 4 + w) * 8192 + lane * 16;
    __builtin_amdgcn_raw_buffer_store_b128(fin, wr, fbase + member * 1024, 0, 16);
    asm volatile("s_waitcnt vmcnt(0)" ::: "memory");
    if (lane == 0) __hip_atomic_store((gu64*)(sync + 128) + member, tag, __ATOMIC_RELAXED, __HIP_MEMORY_SCOPE_AGENT);
    stamp(ws, 5, lane);
    {
        const float kt = halfsum((kp[0] + kp[1]) + (kp[2] + kp[3]));
        const unsigned s2 = r >> 4, jhi = (r >> 3) & 1, hh = (r >> 2) & 1, jlo = r & 3;
        if (h == 0) *(LAS unsigned short*)(lds + L_KS + ((w * 2 + s2) * 2 + hh) * 16 + (4 * jhi + jlo) * 2) = (unsigned short)(pack2(kt, 0.f) & 0xffffu);
    }
    poll8((const gu64*)(sync + 128), tag, lane);
    stamp(ws, 6, lane);
    u32x4 ff[8];
#pragma unroll
    for (int f = 0; f < 8; ++f) ff[f] = __builtin_amdgcn_raw_buffer_load_b128(wr, fbase + f * 1024, 0, 16);
#pragma unroll
    for (int f = 0; f < 8; ++f) *(LAS u32x4*)(lds + L_KV + (w * 8 + f) * 1024 + lane * 16) = ff[f];
    stamp(ws, 7, lane);
    bar();
}

extern "C" void kernel_launch(void* const* d_in, const int* in_sizes, int n_in, void* d_out, int out_size, void* d_ws, size_t ws_size,
                              hipStream_t stream) {
    performer_kernel<<<256, 512, 0, stream>>>((const char*)d_in[0], (const char*)d_in[1], (const char*)d_in[2], (const char*)d_in[3],
                                              (char*)d_out, (char*)d_ws);
}
```

```cpp
#define STAMP 1
#define PROBE_STAT 0
#include <hip/hip_runtime.h>
#include <hip/hip_bf16.h>
#include <stdint.h>

typedef __bf16 bf16x2 __attribute__((ext_vector_type(2)));
typedef __bf16 bf16x8 __attribute__((ext_vector_type(8)));
typedef short s16x4 __attribute__((ext_vector_type(4)));
typedef short s16x8 __attribute__((ext_vector_type(8)));
typedef float f32x2 __attribute__((ext_vector_type(2)));
typedef float f32x16 __attribute__((ext_vector_type(16)));
typedef unsigned u32x2 __attribute__((ext_vector_type(2)));
typedef unsigned u32x4 __attribute__((ext_vector_type(4)));
#define LAS __attribute__((address_space(3)))
#define GAS __attribute__((address_space(1)))
#define DI __device__ __forceinline__
typedef GAS unsigned long long gu64;

#define NTL " nt"

constexpr int kS = 2048;
constexpr float kC1 = 0.29730177875068026f * 1.4426950408889634f;
constexpr float kCN = -0.5f * 0.08838834764831845f * 1.4426950408889634f;
constexpr float kCB = -3.5f;

constexpr unsigned WS_SYNC = 0;
constexpr unsigned WS_KS = 0x10000;
constexpr unsigned WS_F = 0x40000;
constexpr unsigned WS_P = 0x200000;
constexpr unsigned WS_STAMP = 0x1000000;
constexpr unsigned WS_BYTES = 0x1100000;

constexpr unsigned L_PROJ = 0;
constexpr unsigned L_KV = 32768;
constexpr unsigned L_KS = 131072;
constexpr unsigned L_BYTES = 131072 + 256;

constexpr unsigned kSpinCap = 1u << 20;

DI f32x16 mfma(bf16x8 a, bf16x8 b, f32x16 c) { return __builtin_amdgcn_mfma_f32_32x32x16_bf16(a, b, c, 0, 0, 0); }
DI unsigned pack2(float lo, float hi) { f32x2 f = {lo, hi}; return __builtin_bit_cast(unsigned, __builtin_convertvector(f, bf16x2)); }
DI float bflo(unsigned u) { return __uint_as_float(u << 16); }
DI float bfhi(unsigned u) { return __uint_as_float(u & 0xffff0000u); }
DI float dot2(unsigned u, float acc) { bf16x2 a = __builtin_bit_cast(bf16x2, u); return __builtin_amdgcn_fdot2_f32_bf16(a, a, acc, false); }
DI float sumsq(u32x4 v, float acc) { acc = dot2(v[0], acc); acc = dot2(v[1], acc); acc = dot2(v[2], acc); return dot2(v[3], acc); }
DI float halfsum(float x) { auto r = __builtin_amdgcn_permlane32_swap(__float_as_uint(x), __float_as_uint(x), false, false); return __uint_as_float(r[0]) + __uint_as_float(r[1]); }
DI bf16x8 asbf(u32x4 v) { return __builtin_bit_cast(bf16x8, v); }
DI u32x4 pack8(const f32x16& x, int s) {
    u32x4 p; p[0] = pack2(x[8 * s], x[8 * s + 1]); p[1] = pack2(x[8 * s + 2], x[8 * s + 3]); p[2] = pack2(x[8 * s + 4], x[8 * s + 5]); p[3] = pack2(x[8 * s + 6], x[8 * s + 7]); return p;
}
DI void bar() { asm volatile("s_waitcnt lgkmcnt(0)\n\ts_barrier" ::: "memory"); }
DI unsigned swz(unsigned row) { return ((row & 3) << 2) | ((row >> 2) & 3); }
DI void stampc(char* ws, int idx, int lane) {
    unsigned long long t = __builtin_amdgcn_s_memtime();
    if (lane == 0) *(GAS unsigned long long*)(ws + WS_STAMP + ((size_t)blockIdx.x * 8 + (threadIdx.x >> 6)) * 256 + idx * 8) = t;
}
DI void stamp(char* ws, int idx, int lane) {
    unsigned long long t = __builtin_amdgcn_s_memrealtime();
    if (lane == 0) *(GAS unsigned long long*)(ws + WS_STAMP + ((size_t)blockIdx.x * 8 + (threadIdx.x >> 6)) * 256 + idx * 8) = t;
}
DI void poll8(const gu64* line, unsigned long long tag, int lane) {
    for (unsigned spins = 0; spins < kSpinCap; ++spins) {
        unsigned long long x = __hip_atomic_load(line + (lane & 7), __ATOMIC_RELAXED, __HIP_MEMORY_SCOPE_AGENT);
        if (__all(x == tag)) break;
        __builtin_amdgcn_s_sleep(1);
    }
    asm volatile("" ::: "memory");
}

#define STR2(x) #x
#define STR(x) STR2(x)
#define DMA_KV(st) \
    "s_add_u32 m0, %[ldsw], " STR(st) "*16384\n\t" \
    "s_nop 0\n\t" \
    "global_load_lds_dwordx4 %[vo0], %[kb] offset:0" NTL "\n\t" \
    "global_load_lds_dwordx4 %[vo1], %[kb] offset:1024" NTL "\n\t" \
    "s_add_u32 m0, %[ldsw], " STR(st) "*16384+8192\n\t" \
    "s_nop 0\n\t" \
    "global_load_lds_dwordx4 %[vo0], %[vb] offset:0" NTL "\n\t" \
    "global_load_lds_dwordx4 %[vo1], %[vb] offset:1024" NTL "\n\t" \
    "v_add_u32 %[vo0], 0x2000, %[vo0]\n\t" \
    "v_add_u32 %[vo1], 0x2000, %[vo1]\n\t"
#define QLD(r, vq, off) "global_load_dwordx4 %[" #r "], %[" #vq "], %[qb] offset:" STR(off) NTL "\n\t"
#define QG0 QLD(q0, vq0, 0) QLD(q1, vq0, 32) QLD(q2, vq0, 64) QLD(q3, vq0, 96)
#define QG1 QLD(q4, vq0, 128) QLD(q5, vq0, 160) QLD(q6, vq0, 192) QLD(q7, vq0, 224)
#define QG2 QLD(q8, vq1, 0) QLD(q9, vq1, 32) QLD(q10, vq1, 64) QLD(q11, vq1, 96)
#define QG3 QLD(q12, vq1, 128) QLD(q13, vq1, 160) QLD(q14, vq1, 192) QLD(q15, vq1, 224)
#define WB(n) "s_waitcnt vmcnt(" STR(n) ")\n\ts_barrier\n\t"

__global__ __launch_bounds__(512, 2) void performer_kernel(const char* __restrict__ gq, const char* __restrict__ gk, const char* __restrict__ gv,
                                                           const char* __restrict__ gproj, char* __restrict__ gout, char* __restrict__ ws) {
    __shared__ __attribute__((aligned(16))) char lds[L_BYTES];
    const int lane = threadIdx.x & 63, r = lane & 31, h = lane >> 5;
    const int wv = __builtin_amdgcn_readfirstlane(threadIdx.x >> 6), w = wv & 3;
    const int b = blockIdx.x, head = (b & 7) * 4 + (b >> 6), member = (b >> 3) & 7;
    const size_t rowbase = ((size_t)head * kS + member * 256) * 256;
    stamp(ws, 0, lane);
    stampc(ws, 20, lane);

    if (wv >= 4) {
        const unsigned ldsbase = (unsigned)(size_t)(LAS char*)lds;
        const unsigned ldsw = __builtin_amdgcn_readfirstlane(ldsbase + w * 2048);
        const unsigned lr = lane >> 4, sl = lane & 15;
        unsigned vo0 = w * 2048 + lr * 256 + 16 * (sl ^ ((lr << 2) | ((2 * w) & 3)));
        unsigned vo1 = w * 2048 + lr * 256 + 16 * (sl ^ ((lr << 2) | ((2 * w + 1) & 3)));
        const unsigned vq0 = w * 16384 + r * 256 + h * 16, vq1 = vq0 + 8192;
        const char* kb = gk + rowbase; const char* vb = gv + rowbase; const char* qb = gq + rowbase;
        u32x4 q0, q1, q2, q3, q4, q5, q6, q7, q8, q9, q10, q11, q12, q13, q14, q15; unsigned keep;
        asm volatile("s_mov_b32 %[keep], m0\n\t"
                     DMA_KV(0) DMA_KV(1) DMA_KV(2) DMA_KV(3) DMA_KV(4) DMA_KV(5)
                     WB(20) DMA_KV(6) WB(20) DMA_KV(7) WB(20) QG0 WB(20) QG1 WB(20) QG2 WB(20) QG3 WB(20) WB(16)
                     "s_waitcnt vmcnt(0)\n\ts_mov_b32 m0, %[keep]"
                     : [keep] "=&s"(keep), [vo0] "+v"(vo0), [vo1] "+v"(vo1),
                       [q0] "=&v"(q0), [q1] "=&v"(q1), [q2] "=&v"(q2), [q3] "=&v"(q3), [q4] "=&v"(q4), [q5] "=&v"(q5), [q6] "=&v"(q6), [q7] "=&v"(q7),
                       [q8] "=&v"(q8), [q9] "=&v"(q9), [q10] "=&v"(q10), [q11] "=&v"(q11), [q12] "=&v"(q12), [q13] "=&v"(q13), [q14] "=&v"(q14), [q15] "=&v"(q15)
                     : [ldsw] "s"(ldsw), [kb] "s"(kb), [vb] "s"(vb), [qb] "s"(qb), [vq0] "v"(vq0), [vq1] "v"(vq1)
                     : "memory", "scc");
        stamp(ws, 1, lane);
        const u32x4 qf[2][8] = {{q0, q1, q2, q3, q4, q5, q6, q7}, {q8, q9, q10, q11, q12, q13, q14, q15}};
        f32x16 cin[2];
#pragma unroll
        for (int u = 0; u < 2; ++u) {
            float n = 0.f;
#pragma unroll
            for (int t = 0; t < 8; ++t) n = sumsq(qf[u][t], n);
            const float c = halfsum(n) * kCN + kCB;
#pragma unroll
            for (int i = 0; i < 16; ++i) cin[u][i] = c;
        }
        u32x4 pq[2][4][2];
#pragma unroll
        for (int mt = 0; mt < 4; ++mt) {
            f32x16 x0 = cin[0], x1 = cin[1];
#pragma unroll
            for (int t = 0; t < 8; ++t) {
                const bf16x8 a = *(const LAS bf16x8*)(lds + L_PROJ + (mt * 8 + t) * 1024 + lane * 16);
                x0 = mfma(a, asbf(qf[0][t]), x0);
                x1 = mfma(a, asbf(qf[1][t]), x1);
            }
#pragma unroll
            for (int i = 0; i < 16; ++i) { x0[i] = __builtin_amdgcn_exp2f(x0[i]); x1[i] = __builtin_amdgcn_exp2f(x1[i]); }
            pq[0][mt][0] = pack8(x0, 0); pq[0][mt][1] = pack8(x0, 1);
            pq[1][mt][0] = pack8(x1, 0); pq[1][mt][1] = pack8(x1, 1);
        }
        stamp(ws, 2, lane);
        bar();
        stamp(ws, 3, lane);
        f32x16 dn0 = {}, dn1 = {};
#pragma unroll
        for (int k = 0; k < 8; ++k) {
            const bf16x8 a = *(const LAS bf16x8*)(lds + L_KS + k * 32 + h * 16);
            dn0 = mfma(a, asbf(pq[0][k >> 1][k & 1]), dn0);
            dn1 = mfma(a, asbf(pq[1][k >> 1][k & 1]), dn1);
        }
        const float rd[2] = {__builtin_amdgcn_rcpf(dn0[0] + 1e-6f), __builtin_amdgcn_rcpf(dn1[0] + 1e-6f)};
        char* ob = gout + rowbase + (size_t)(w * 64 + r) * 256 + h * 16;
#pragma unroll
        for (int c = 0; c < 4; ++c) {
            f32x16 o[2] = {{}, {}};
#pragma unroll
            for (int k = 0; k < 8; ++k) {
                const bf16x8 a = *(const LAS bf16x8*)(lds + L_KV + ((k >> 1) * 8 + c * 2 + (k & 1)) * 1024 + lane * 16);
                o[0] = mfma(a, asbf(pq[0][k >> 1][k & 1]), o[0]);
                o[1] = mfma(a, asbf(pq[1][k >> 1][k & 1]), o[1]);
            }
#pragma unroll
            for (int u = 0; u < 2; ++u) {
#pragma unroll
                for (int g = 0; g < 4; g += 2) {
                    const unsigned ax = pack2(o[u][4 * g] * rd[u], o[u][4 * g + 1] * rd[u]), ay = pack2(o[u][4 * g + 2] * rd[u], o[u][4 * g + 3] * rd[u]);
                    const unsigned bx = pack2(o[u][4 * g + 4] * rd[u], o[u][4 * g + 5] * rd[u]), by = pack2(o[u][4 * g + 6] * rd[u], o[u][4 * g + 7] * rd[u]);
                    const auto sx = __builtin_amdgcn_permlane32_swap(ax, bx, false, false);
                    const auto sy = __builtin_amdgcn_permlane32_swap(ay, by, false, false);
                    const u32x4 v = {sx[0], sy[0], sx[1], sy[1]};
                    *(GAS u32x4*)(ob + u * 8192 + c * 64 + g * 16) = v;
                }
            }
        }
        stamp(ws, 4, lane);
        stampc(ws, 21, lane);
        return;
    }

    char* sync = ws + WS_SYNC + (head * 4 + w) * 256;
    const unsigned long long tag = __hip_atomic_load((gu64*)(sync + 64), __ATOMIC_RELAXED, __HIP_MEMORY_SCOPE_AGENT) + 1ull;
    u32x4 pb[8];
#pragma unroll
    for (int t = 0; t < 8; ++t) pb[t] = *(const GAS u32x4*)(gproj + (32 * w + r) * 256 + 32 * t + 16 * h);
    asm volatile("" : "+v"(pb[0]), "+v"(pb[1]), "+v"(pb[2]), "+v"(pb[3]), "+v"(pb[4]), "+v"(pb[5]), "+v"(pb[6]), "+v"(pb[7]));
#pragma unroll
    for (int t = 0; t < 8; ++t)
#pragma unroll
        for (int i = 0; i < 4; ++i) pb[t][i] = pack2(bflo(pb[t][i]) * kC1, bfhi(pb[t][i]) * kC1);
    unsigned ka[8], va[2][4];
#pragma unroll
    for (int t = 0; t < 8; ++t) ka[t] = 256 * r + 16 * ((2 * t + h) ^ swz(r));
    {
        const unsigned q = (lane & 15) >> 2, p = lane & 3, blk = (lane >> 4) & 1;
#pragma unroll
        for (int t2 = 0; t2 < 2; ++t2)
#pragma unroll
            for (int c = 0; c < 4; ++c) {
                const unsigned row = 8 * t2 + 4 * h + q;
                va[t2][c] = 8192 + 256 * row + 16 * ((4 * c + 2 * blk + (p >> 1)) ^ swz(row)) + 8 * (p & 1);
            }
    }
    const u32x4 ones = {0x3f803f80u, 0u, 0u, 0u};
    f32x16 kv[4] = {{}, {}, {}, {}};
    float ks0 = 0.f, ks1 = 0.f, ks2 = 0.f, ks3 = 0.f;
#pragma unroll
    for (int st = 0; st < 8; ++st) {
        bar();
        if (st == 0) stamp(ws, 1, lane);
        stamp(ws, 8 + st, lane);
        if (st == 2) {
#pragma unroll
            for (int t = 0; t < 8; ++t) *(LAS u32x4*)(lds + L_PROJ + (w * 8 + t) * 1024 + lane * 16) = pb[t];
        }
        u32x4 kf[8];
#pragma unroll
        for (int t = 0; t < 8; ++t) kf[t] = *(const LAS u32x4*)(lds + st * 16384 + ka[t]);
        s16x4 vf[2][4][2];
#pragma unroll
        for (int ss = 0; ss < 2; ++ss)
#pragma unroll
            for (int c = 0; c < 4; ++c)
#pragma unroll
                for (int t2 = 0; t2 < 2; ++t2)
                    vf[ss][c][t2] = __builtin_amdgcn_ds_read_tr16_b64_v4i16((LAS s16x4*)(lds + st * 16384 + ss * 4096 + va[t2][c]));
        __builtin_amdgcn_sched_barrier(0);
        f32x16 x = {};
        float n0 = 0.f, n1 = 0.f;
#pragma unroll
        for (int t = 0; t < 8; ++t) {
            x = mfma(asbf(kf[t]), asbf(pb[t]), x);
            n0 = dot2(kf[t][0], n0); n1 = dot2(kf[t][1], n1); n0 = dot2(kf[t][2], n0); n1 = dot2(kf[t][3], n1);
        }
        const float cpart = (n0 + n1) * kCN + (h ? 0.f : kCB);
        const unsigned chi = pack2(cpart, 0.f) & 0xffffu;
        const unsigned cpk = pack2(bflo(chi), cpart - bflo(chi));
        const u32x4 an = {cpk, 0u, 0u, 0u};
        x = mfma(asbf(an), asbf(ones), x);
#pragma unroll
        for (int i = 0; i < 16; ++i) x[i] = __builtin_amdgcn_exp2f(x[i]);
#pragma unroll
        for (int ss = 0; ss < 2; ++ss) {
            const u32x4 a = pack8(x, ss);
#pragma unroll
            for (int c = 0; c < 4; ++c) {
                const s16x8 bv = __builtin_shufflevector(vf[ss][c][0], vf[ss][c][1], 0, 1, 2, 3, 4, 5, 6, 7);
                kv[c] = mfma(asbf(a), __builtin_bit_cast(bf16x8, bv), kv[c]);
            }
        }
#pragma unroll
        for (int i = 0; i < 16; i += 4) { ks0 += x[i]; ks1 += x[i + 1]; ks2 += x[i + 2]; ks3 += x[i + 3]; }
        asm volatile("" : "+v"(ks0), "+v"(ks1), "+v"(ks2), "+v"(ks3));
    }
    stamp(ws, 2, lane);
    const __amdgpu_buffer_rsrc_t wr = __builtin_amdgcn_make_buffer_rsrc(ws, 0, WS_BYTES, 0x00020000);
    const unsigned pbase = WS_P + ((head * 8 + member) * 4 + w) * 8192 + lane * 16;
#pragma unroll
    for (int c = 0; c < 4; ++c)
#pragma unroll
        for (int s = 0; s < 2; ++s) __builtin_amdgcn_raw_buffer_store_b128(pack8(kv[c], s), wr, pbase + (c * 2 + s) * 1024, 0, 16);
    const float ks = halfsum((ks0 + ks1) + (ks2 + ks3));
    if (h == 0) __builtin_amdgcn_raw_buffer_store_b32(__float_as_uint(ks), wr, WS_KS + (head * 8 + member) * 512 + (32 * w + r) * 4, 0, 16);
    asm volatile("s_waitcnt vmcnt(0)" ::: "memory");
    if (lane == 0) __hip_atomic_store((gu64*)sync + member, tag, __ATOMIC_RELAXED, __HIP_MEMORY_SCOPE_AGENT);
    stamp(ws, 3, lane);
    poll8((const gu64*)sync, tag, lane);
    stamp(ws, 4, lane);
    if (member == 0 && lane == 0) __hip_atomic_store((gu64*)(sync + 64), tag, __ATOMIC_RELAXED, __HIP_MEMORY_SCOPE_AGENT);
    u32x4 part[8];
#pragma unroll
    for (int jj = 0; jj < 8; ++jj) part[jj] = __builtin_amdgcn_raw_buffer_load_b128(wr, WS_P + ((head * 8 + jj) * 4 + w) * 8192 + member * 1024 + lane * 16, 0, 16);
    float kp[4];
#pragma unroll
    for (int i = 0; i < 4; ++i) kp[i] = __uint_as_float(__builtin_amdgcn_raw_buffer_load_b32(wr, WS_KS + (head * 8 + 4 * h + i) * 512 + (32 * w + r) * 4, 0, 16));
    u32x4 fin;
#pragma unroll
    for (int i = 0; i < 4; ++i) {
        float lo = 0.f, hi = 0.f;
#pragma unroll
        for (int jj = 0; jj < 8; ++jj) { lo += bflo(part[jj][i]); hi += bfhi(part[jj][i]); }
        fin[i] = pack2(lo, hi);
    }
    const unsigned fbase = WS_F + (head * 4 + w) * 8192 + lane * 16;
    __builtin_amdgcn_raw_buffer_store_b128(fin, wr, fbase + member * 1024, 0, 16);
    asm volatile("s_waitcnt vmcnt(0)" ::: "memory");
    if (lane == 0) __hip_atomic_store((gu64*)(sync + 128) + member, tag, __ATOMIC_RELAXED, __HIP_MEMORY_SCOPE_AGENT);
    stamp(ws, 5, lane);
    {
        const float kt = halfsum((kp[0] + kp[1]) + (kp[2] + kp[3]));
        const unsigned s2 = r >> 4, jhi = (r >> 3) & 1, hh = (r >> 2) & 1, jlo = r & 3;
        if (h == 0) *(LAS unsigned short*)(lds + L_KS + ((w * 2 + s2) * 2 + hh) * 16 + (4 * jhi + jlo) * 2) = (unsigned short)(pack2(kt, 0.f) & 0xffffu);
    }
    poll8((const gu64*)(sync + 128), tag, lane);
    stamp(ws, 6, lane);
    u32x4 ff[8];
#pragma unroll
    for (int f = 0; f < 8; ++f) ff[f] = __builtin_amdgcn_raw_buffer_load_b128(wr, fbase + f * 1024, 0, 16);
#pragma unroll
    for (int f = 0; f < 8; ++f) *(LAS u32x4*)(lds + L_KV + (w * 8 + f) * 1024 + lane * 16) = ff[f];
    stamp(ws, 7, lane);
    bar();
}

__global__ __launch_bounds__(256) void k_probe(char* ws) {
    __shared__ unsigned long long red[2][256];
    const unsigned long long tb = __builtin_amdgcn_s_memrealtime();
    unsigned long long t0 = ~0ull, x = 0;
    const int b = threadIdx.x;
    for (int w = 0; w < 8; ++w) {
        const unsigned long long* p = (const unsigned long long*)(ws + WS_STAMP + ((size_t)b * 8 + w) * 256);
        t0 = p[0] < t0 ? p[0] : t0;
        unsigned long long y = 0;
        if ((PROBE_STAT == 1 || PROBE_STAT == 7 || PROBE_STAT == 8) && w >= 4) y = p[4];
        if (PROBE_STAT == 2) y = p[0];
        if (PROBE_STAT == 3 && w < 4) y = p[1];
        if (PROBE_STAT == 4 && w < 4) y = p[2];
        if (PROBE_STAT == 5 && w < 4) y = p[7];
        if (PROBE_STAT == 6 && w < 4) y = p[3];
        x = y > x ? y : x;
    }
    red[0][b] = t0; red[1][b] = x;
    __syncthreads();
    if (b == 0) {
        for (int i = 1; i < 256; ++i) { t0 = red[0][i] < t0 ? red[0][i] : t0; x = red[1][i] > x ? red[1][i] : x; }
        unsigned long long d = PROBE_STAT == 0 ? 0 : (x - t0) * 10;
        if (PROBE_STAT == 7) d = (tb - x) * 10;
        if (PROBE_STAT == 8) d = (__builtin_amdgcn_s_memrealtime() - tb) * 10;
        if (d > 100000) d = 100000;
        unsigned long long s0 = __builtin_amdgcn_s_memrealtime();
        while (__builtin_amdgcn_s_memrealtime() - s0 < d) __builtin_amdgcn_s_sleep(2);
    }
}
extern "C" void kernel_launch(void* const* d_in, const int* in_sizes, int n_in, void* d_out, int out_size, void* d_ws, size_t ws_size,
                              hipStream_t stream) {
    performer_kernel<<<256, 512, 0, stream>>>((const char*)d_in[0], (const char*)d_in[1], (const char*)d_in[2], (const char*)d_in[3],
                                              (char*)d_out, (char*)d_ws);
    k_probe<<<1, 256, 0, stream>>>((char*)d_ws);
}
```

```cpp
#include <hip/hip_runtime.h>
#include <hip/hip_bf16.h>
#include <stdint.h>
#include <utility>

typedef __bf16 bf16x2 __attribute__((ext_vector_type(2)));
typedef __bf16 bf16x8 __attribute__((ext_vector_type(8)));
typedef short s16x4 __attribute__((ext_vector_type(4)));
typedef float f32x2 __attribute__((ext_vector_type(2)));
typedef float f32x16 __attribute__((ext_vector_type(16)));
typedef unsigned u32x2 __attribute__((ext_vector_type(2)));
typedef unsigned u32x4 __attribute__((ext_vector_type(4)));
#define LAS __attribute__((address_space(3)))
#define GAS __attribute__((address_space(1)))
#define DI __device__ __forceinline__
typedef GAS unsigned long long gu64;

constexpr int kS = 2048;
constexpr float kC1 = 0.29730177875068026f * 1.4426950408889634f;
constexpr float kCN = -0.5f * 0.08838834764831845f * 1.4426950408889634f;
constexpr float kCB = -3.5f;

constexpr unsigned WS_HEAD = 0x80000;
constexpr unsigned WS_SYNC = 0;
constexpr unsigned WS_KS = 0x2000;
constexpr unsigned WS_F = 0x8000;
constexpr unsigned WS_P = 0x10000;
constexpr unsigned WS_BYTES = 32 * WS_HEAD;

constexpr unsigned L_Q = 1 * 16384;
constexpr unsigned L_KV = 5 * 16384;
constexpr unsigned L_KS = 7 * 16384;
constexpr unsigned L_FLAG = 7 * 16384 + 256;
constexpr unsigned L_AN = 7 * 16384 + 1024;
constexpr unsigned L_SCR = 7 * 16384 + 8192;
constexpr unsigned L_PROJ = 8 * 16384;
constexpr unsigned L_BYTES = 10 * 16384;

constexpr unsigned kSpinCap = 1u << 20;
constexpr unsigned kSent = 0xffffffffu;
constexpr unsigned kOut = 0x40000000u;
constexpr int kSc1 = 16, kNt = 2;

DI f32x16 mfma(bf16x8 a, bf16x8 b, f32x16 c) { return __builtin_amdgcn_mfma_f32_32x32x16_bf16(a, b, c, 0, 0, 0); }
DI unsigned pack2(float lo, float hi) { f32x2 f = {lo, hi}; return __builtin_bit_cast(unsigned, __builtin_convertvector(f, bf16x2)); }
DI float bflo(unsigned u) { return __uint_as_float(u << 16); }
DI float bfhi(unsigned u) { return __uint_as_float(u & 0xffff0000u); }
DI float sq2(unsigned u, float acc) { bf16x2 a = __builtin_bit_cast(bf16x2, u); return __builtin_amdgcn_fdot2_f32_bf16(a, a, acc, false); }
DI float halfsum(float x) { auto r = __builtin_amdgcn_permlane32_swap(__float_as_uint(x), __float_as_uint(x), false, false); return __uint_as_float(r[0]) + __uint_as_float(r[1]); }
DI bf16x8 asbf(u32x4 v) { return __builtin_bit_cast(bf16x8, v); }
DI bf16x8 cat(s16x4 lo, s16x4 hi) { return __builtin_bit_cast(bf16x8, __builtin_shufflevector(lo, hi, 0, 1, 2, 3, 4, 5, 6, 7)); }
DI u32x4 pack8(const f32x16& x, int s) {
    return u32x4{pack2(x[8 * s], x[8 * s + 1]), pack2(x[8 * s + 2], x[8 * s + 3]), pack2(x[8 * s + 4], x[8 * s + 5]), pack2(x[8 * s + 6], x[8 * s + 7])};
}
DI u32x4 norm_frag(float n, bool bias) {
    const float c = n * kCN + (bias ? kCB : 0.f);
    const unsigned hi = pack2(c, 0.f) & 0xffffu;
    return u32x4{pack2(bflo(hi), c - bflo(hi)), 0u, 0u, 0u};
}
template <int CTRL> DI float dppf(float x) { return __int_as_float(__builtin_amdgcn_update_dpp(0, __float_as_int(x), CTRL, 0xf, 0xf, true)); }
template <int... I, class F> DI void static_for_impl(std::integer_sequence<int, I...>, F&& f) { (f(std::integral_constant<int, I>{}), ...); }
template <int N, class F> DI void static_for(F&& f) { static_for_impl(std::make_integer_sequence<int, N>{}, f); }

DI void fence() { __builtin_amdgcn_sched_barrier(0); }
template <class T> DI void pin(T& x) { asm volatile("" : "+v"(x)); }
DI void wait_lds() { __builtin_amdgcn_s_waitcnt(0xc07f); asm volatile("" ::: "memory"); }
template <int N> DI void waitv() { asm volatile("s_waitcnt vmcnt(%0)" :: "i"(N) : "memory"); }

DI unsigned min4(u32x4 f) { return __builtin_amdgcn_readfirstlane(min(min(f[0], f[1]), min(f[2], f[3]))); }
template <int SLEEP> DI void flags_wait(const LAS char* p, unsigned need) {
#pragma unroll 1
    for (unsigned spins = 0; spins < kSpinCap; ++spins) {
        if (min4(*(const volatile LAS u32x4*)p) >= need) break;
        if (SLEEP) __builtin_amdgcn_s_sleep(SLEEP);
    }
    asm volatile("" ::: "memory");
}
DI void flag_set(LAS char* p, unsigned v, int lane) {
    wait_lds();
    if (lane == 0) *(volatile LAS unsigned*)p = v;
}

template <bool STREAM> DI void dma_rows(unsigned ldsaddr, unsigned voff, const char* base) {
    unsigned keep;
    if (STREAM)
        asm volatile("s_mov_b32 %0, m0\n\ts_mov_b32 m0, %1\n\ts_nop 0\n\tglobal_load_lds_dwordx4 %2, %3 offset:0 nt\n\t"
                     "s_add_u32 m0, %1, 896\n\ts_nop 0\n\tglobal_load_lds_dwordx4 %2, %3 offset:128 nt\n\ts_mov_b32 m0, %0"
                     : "=&s"(keep) : "s"(ldsaddr), "v"(voff), "s"(base) : "memory", "scc");
    else
        asm volatile("s_mov_b32 %0, m0\n\ts_mov_b32 m0, %1\n\ts_nop 0\n\tglobal_load_lds_dwordx4 %2, %3 offset:0\n\t"
                     "s_add_u32 m0, %1, 896\n\ts_nop 0\n\tglobal_load_lds_dwordx4 %2, %3 offset:128\n\ts_mov_b32 m0, %0"
                     : "=&s"(keep) : "s"(ldsaddr), "v"(voff), "s"(base) : "memory", "scc");
}

__global__ __launch_bounds__(512, 2) void performer_kernel(const char* __restrict__ gq, const char* __restrict__ gk, const char* __restrict__ gv,
                                                           const char* __restrict__ gproj, char* gout, char* ws) {
    __shared__ __attribute__((aligned(16))) char lds[L_BYTES];
    const int lane = threadIdx.x & 63, r = lane & 31, h = lane >> 5;
    const int wv = __builtin_amdgcn_readfirstlane(threadIdx.x >> 6), w = wv & 3;
    const int b = blockIdx.x, head = (b & 7) * 4 + (b >> 6), member = (b >> 3) & 7;
    const size_t rowbase = ((size_t)head * kS + member * 256) * 256;
    const unsigned ldsbase = (unsigned)(size_t)(LAS char*)lds;
    {
        unsigned long long pc; unsigned len, len2, keep;
        asm volatile("s_getpc_b64 %0\n.Lperf_here:\n\ts_mov_b32 %1, .Lperf_end_q-.Lperf_here\n\ts_mov_b32 %2, .Lperf_end_kv-.Lperf_here" : "=s"(pc), "=s"(len), "=s"(len2));
        len = max(len, len2);
        const unsigned off = min((unsigned)((wv * 64 + lane) * 128), len - 4u) & ~3u;
        if (wv * 8192 < (int)len)
            asm volatile("s_mov_b32 %0, m0\n\ts_mov_b32 m0, %1\n\ts_nop 0\n\tglobal_load_lds_dword %2, %3\n\ts_mov_b32 m0, %0"
                         : "=&s"(keep) : "s"(ldsbase + L_SCR + wv * 256), "v"(off), "s"(pc) : "memory");
    }
    const unsigned lrow = (lane >> 2) & 7, cg = lane >> 5, sl = lane & 3, xb = (lane >> 4) & 1;
    unsigned dve = lrow * 256 + cg * 64 + 16 * (sl ^ xb), dvo = lrow * 256 + cg * 64 + 16 * (sl ^ (2 + xb));
    unsigned vo = w * 2048 + ((w & 1) ? dvo : dve);
    pin(dve); pin(dvo); pin(vo);

    asm volatile("" :: "s"(gq), "s"(gk), "s"(gv), "s"(gproj), "s"(gout), "s"(ws));
    const char* kb = gk + rowbase; const char* vb = gv + rowbase; const char* qb = gq + rowbase;
    const unsigned lk = __builtin_amdgcn_readfirstlane(ldsbase + w * 2048);
    auto kvg = [&](int st) { const unsigned slot = (st % 7) * 16384; dma_rows<true>(lk + slot, vo + st * 8192, kb); dma_rows<true>(lk + slot + 8192, vo + st * 8192, vb); };
    char* sync = ws + head * WS_HEAD + WS_SYNC + w * 2048;
    const __amdgpu_buffer_rsrc_t wr = __builtin_amdgcn_make_buffer_rsrc(ws, 0, WS_BYTES, 0x00020000);
    const unsigned fbase = head * WS_HEAD + WS_F + w * 8192 + lane * 16;
    if (wv >= 4) { kvg(0); kvg(1); }
    else {
        const unsigned lp = __builtin_amdgcn_readfirstlane(ldsbase + L_PROJ + w * 8192);
#pragma unroll
        for (int rg = 0; rg < 4; ++rg) dma_rows<false>(lp + rg * 2048, w * 8192 + rg * 2048 + ((rg & 1) ? dvo : dve), gproj);
        __builtin_amdgcn_raw_buffer_store_b128(u32x4{kSent, kSent, kSent, kSent}, wr, fbase + member * 1024, 0, kSc1);
    }
    unsigned long long tag = __hip_atomic_load((gu64*)(sync + 1024), __ATOMIC_RELAXED, __HIP_MEMORY_SCOPE_AGENT);
    LAS char* const flags = (LAS char*)lds + L_FLAG;
    if (lane < 4) *(volatile LAS unsigned*)(flags + lane * 16 + w * 4) = 0u;
    asm volatile("s_waitcnt lgkmcnt(0)\n\ts_barrier" ::: "memory");
    const unsigned ra0 = 2048 * (r >> 3) + 64 * (r & 7) + 16 * (h ^ ((r >> 2) & 3));
    const unsigned ra1 = 2048 * (r >> 3) + 64 * (r & 7) + 16 * ((2 + h) ^ ((r >> 2) & 3));
    const u32x4 ones = {0x3f803f80u, 0u, 0u, 0u};
    int role = wv;
    asm volatile("" : "+s"(role));

    if (role >= 4) {
        const unsigned lq = __builtin_amdgcn_readfirstlane(ldsbase + L_Q + w * 16384);
        auto qg = [&](int i) { dma_rows<true>(lq + i * 4096, w * 16384 + i * 4096 + dve, qb); dma_rows<true>(lq + i * 4096 + 2048, w * 16384 + i * 4096 + 2048 + dvo, qb); };
        const unsigned nra = w * 2048 + (lane >> 3) * 64 + ((lane & 7) >> 1) * 512 + (lane & 1) * 32;
        const unsigned nwa = L_AN + ((lane & 7) == 0 ? 0 : 128) + (w * 8 + (lane >> 3)) * 4;
        auto norms = [&](int st) {
            const LAS char* p = (const LAS char*)lds + (st % 7) * 16384 + nra;
            const u32x4 a = *(const LAS u32x4*)p, c = *(const LAS u32x4*)(p + 16);
            float n0 = sq2(a[0], 0.f), n1 = sq2(a[1], 0.f);
            n0 = sq2(a[2], n0); n1 = sq2(a[3], n1); n0 = sq2(c[0], n0); n1 = sq2(c[1], n1); n0 = sq2(c[2], n0); n1 = sq2(c[3], n1);
            float n = n0 + n1;
            n += dppf<0xB1>(n); n += dppf<0x4E>(n); n += dppf<0x141>(n);
            const unsigned v = norm_frag(n, true)[0];
            if ((lane & 7) < 2) *(LAS unsigned*)(lds + nwa + st * 256) = (lane & 7) == 0 ? v : 0u;
            asm volatile("" ::: "memory");
        };
        auto landed = [&](int st) { if (lane == 0) *(volatile LAS unsigned*)(flags + w * 4) = st + 1; };
        auto issue = [&](int g) {
            if (g == 7) flags_wait<0>(flags + 16, 1);
            if (g == 8) flags_wait<0>(flags + 16, w + 2);
            if (g < 8) kvg(g); else qg(g - 8);
        };
        static_for<8>([&](auto Sc) {
            constexpr int st = Sc;
            if constexpr (st < 6) {
                waitv<4>();
                issue(st + 2);
                norms(st);
                landed(st);
            } else {
                waitv<6>();
                norms(st);
                waitv<4>();
                landed(st);
                issue(st + 2);
            }
        });
        waitv<4>(); issue(10);
        waitv<4>(); issue(11);
        waitv<0>();
        const LAS char* qs = (const LAS char*)lds + L_Q + w * 16384;
        unsigned pjo = L_PROJ + lane * 16; pin(pjo);
        const LAS char* pj = (const LAS char*)lds + pjo;
        u32x4 qf[2][8];
#pragma unroll
        for (int u = 0; u < 2; ++u)
#pragma unroll
            for (int t = 0; t < 8; ++t) qf[u][t] = *(const LAS u32x4*)(qs + u * 8192 + ((t & 1) ? ra1 : ra0) + 512 * (t >> 1));
        u32x4 pa[4];
#pragma unroll
        for (int i = 0; i < 3; ++i) pa[i] = *(const LAS u32x4*)(pj + i * 1024);
        u32x4 an[2];
#pragma unroll
        for (int u = 0; u < 2; ++u) {
            float n0 = 0.f, n1 = 0.f;
#pragma unroll
            for (int t = 0; t < 8; ++t) { n0 = sq2(qf[u][t][0], n0); n1 = sq2(qf[u][t][1], n1); n0 = sq2(qf[u][t][2], n0); n1 = sq2(qf[u][t][3], n1); }
            an[u] = norm_frag(n0 + n1, h == 0);
        }
        fence();
        u32x4 pq[2][4][2];
        f32x16 x[2][2];
        static_for<72 + 18>([&](auto Gc) {
            constexpr int G = Gc, mt = G / 18, t = (G % 18) >> 1, u = G & 1;
            if constexpr (G < 72) {
                if constexpr (t == 0) x[mt & 1][u] = mfma(asbf(ones), asbf(an[u]), f32x16{});
                else {
                    constexpr int i = mt * 8 + t - 1;
                    if constexpr (u == 0 && i + 3 < 32) pa[(i + 3) & 3] = *(const LAS u32x4*)(pj + (i + 3) * 1024);
                    x[mt & 1][u] = mfma(asbf(pa[i & 3]), asbf(qf[u][t - 1]), x[mt & 1][u]);
                }
            }
            constexpr int E = G - 20;
            if constexpr (E >= 0 && E % 18 < 16 && E / 18 < 4) {
                constexpr int me = E / 18, pr = E % 18, ue = pr >> 3, k = pr & 7;
                const f32x16& p = x[me & 1][ue];
                unsigned v = pack2(__builtin_amdgcn_exp2f(p[2 * k]), __builtin_amdgcn_exp2f(p[2 * k + 1]));
                pin(v);
                pq[ue][me][k >> 2][k & 3] = v;
            }
            fence();
        });
        flags_wait<1>(flags + 32, 1);
        float rd[2];
        {
            u32x4 ka[8];
#pragma unroll
            for (int k = 0; k < 8; ++k) ka[k] = *(const LAS u32x4*)(lds + L_KS + k * 32 + h * 16);
            fence();
            f32x16 dn0 = {}, dn1 = {};
#pragma unroll
            for (int k = 0; k < 8; ++k) {
                dn0 = mfma(asbf(ka[k]), asbf(pq[0][k >> 1][k & 1]), dn0);
                dn1 = mfma(asbf(ka[k]), asbf(pq[1][k >> 1][k & 1]), dn1);
            }
            rd[0] = __builtin_amdgcn_rcpf(dn0[0] + 1e-6f); rd[1] = __builtin_amdgcn_rcpf(dn1[0] + 1e-6f);
            pin(rd[0]); pin(rd[1]);
        }
        const __amdgpu_buffer_rsrc_t orsrc = __builtin_amdgcn_make_buffer_rsrc(gout + rowbase, 0, 65536, 0x00020000);
        const unsigned gr = ((r & 7) << 1) | ((r >> 3) & 1);
        const unsigned rrow = lane >> 3, rch = lane & 7;
        unsigned rda = L_Q + w * 16384 + 128 * rrow + 16 * (rch ^ rrow);
        unsigned oo = (w * 64 + rrow) * 256 + rch * 16;
        unsigned sa[8];
#pragma unroll
        for (int i = 0; i < 8; ++i) { sa[i] = L_Q + w * 16384 + 128 * r + 8 * ((2 * i + h) ^ gr); pin(sa[i]); }
        unsigned fko = L_KV + lane * 16;
        pin(rda); pin(oo); pin(fko);
        const LAS char* fk = (const LAS char*)lds + fko;
        auto faddr = [&](int c, int k) { return fk + ((k >> 1) * 8 + c * 2 + (k & 1)) * 1024; };
        flags_wait<1>(flags + 48, 1);
        u32x4 fa[2][8];
#pragma unroll
        for (int k = 0; k < 8; ++k) fa[0][k] = *(const LAS u32x4*)faddr(0, k);
        f32x16 o[3];
        u32x4 rb[4];
        unsigned pk[2];
        auto rb_read = [&](int uu, int hf, int i) { rb[i] = *(const LAS u32x4*)((const LAS char*)lds + rda + hf * 8192 + uu * 4096 + i * 1024); };
        auto rb_store = [&](int uu, int hf, int i) {
            const u32x4 t = rb[i];
            const u32x4 v = (i & 1) ? u32x4{t[2], t[3], t[0], t[1]} : t;
            __builtin_amdgcn_raw_buffer_store_b128(v, orsrc, oo + (uu * 32 + i * 8) * 256 + hf * 128, 0, kNt);
        };
        auto epi = [&](int jj, int k) {
            const int cp = 2 * (jj >> 2) + (jj & 1), uu = (jj >> 1) & 1;
            const f32x16& t = o[jj % 3];
            pk[k & 1] = pack2(t[2 * k] * rd[uu], t[2 * k + 1] * rd[uu]);
            if (k & 1) *(LAS u32x2*)((LAS char*)lds + sa[4 * (cp & 1) + (k >> 1)] + (cp >> 1) * 8192 + uu * 4096) = u32x2{pk[0], pk[1]};
            else pin(pk[0]);
        };
        static_for<74>([&](auto Gc) {
            constexpr int G = Gc, jj = G >> 3, k = G & 7, c = 2 * (jj >> 2) + (jj & 1), u = (jj >> 1) & 1;
            if constexpr (G < 64) {
                o[jj % 3] = mfma(asbf(fa[c & 1][k]), asbf(pq[u][k >> 1][k & 1]), k == 0 ? f32x16{} : o[jj % 3]);
                if constexpr (jj == 0) fa[1][k] = *(const LAS u32x4*)faddr(1, k);
                if constexpr (jj == 2 || jj == 3) fa[c & 1][k] = *(const LAS u32x4*)faddr(c + 2, k);
            }
            if constexpr (G >= 26 && G < 30) rb_read(0, 0, G - 26);
            if constexpr (G >= 30 && G < 34) rb_store(0, 0, G - 30);
            if constexpr (G >= 42 && G < 46) rb_read(1, 0, G - 42);
            if constexpr (G >= 46 && G < 50) rb_store(1, 0, G - 46);
            if constexpr (G >= 58 && G < 62) rb_read(0, 1, G - 58);
            if constexpr (G >= 62 && G < 66) rb_store(0, 1, G - 62);
            if constexpr (G >= 10) epi((G - 10) >> 3, (G - 10) & 7);
            fence();
        });
#pragma unroll
        for (int i = 0; i < 4; ++i) rb_read(1, 1, i);
#pragma unroll
        for (int i = 0; i < 4; ++i) rb_store(1, 1, i);
        asm volatile(".Lperf_end_q:");
        return;
    }

    waitv<2>();
    u32x4 pb[8];
#pragma unroll
    for (int t = 0; t < 8; ++t) pb[t] = *(const LAS u32x4*)(lds + L_PROJ + w * 8192 + ((t & 1) ? ra1 : ra0) + 512 * (t >> 1));
    asm volatile("" : "+v"(pb[0]), "+v"(pb[1]), "+v"(pb[2]), "+v"(pb[3]), "+v"(pb[4]), "+v"(pb[5]), "+v"(pb[6]), "+v"(pb[7]));
#pragma unroll
    for (int t = 0; t < 8; ++t) {
#pragma unroll
        for (int i = 0; i < 4; ++i) pb[t][i] = pack2(bflo(pb[t][i]) * kC1, bfhi(pb[t][i]) * kC1);
        *(LAS u32x4*)(lds + L_PROJ + (w * 8 + t) * 1024 + lane * 16) = pb[t];
    }
    unsigned va0, va1;
    {
        const unsigned q = (lane & 15) >> 2, p = lane & 3, blk = (lane >> 4) & 1;
        va0 = 8192 + 64 * (4 * h + q) + 16 * ((2 * blk + (p >> 1)) ^ h) + 8 * (p & 1);
        va1 = 8192 + 2048 + 64 * (4 * h + q) + 16 * ((2 * blk + (p >> 1)) ^ (2 + h)) + 8 * (p & 1);
    }
    f32x16 kv[4] = {{}, {}, {}, {}};
    float ks[4] = {0.f, 0.f, 0.f, 0.f};
    f32x16 x;
#pragma unroll
    for (int i = 0; i < 16; ++i) x[i] = -1.0e30f;
    float ea[8] = {0.f, 0.f, 0.f, 0.f, 0.f, 0.f, 0.f, 0.f};
    u32x4 a0 = {0u, 0u, 0u, 0u};
    s16x4 v0[4][2], vA[4][2], vB[4][2];
#pragma unroll
    for (int i = 0; i < 8; ++i) v0[i >> 1][i & 1] = vA[i >> 1][i & 1] = s16x4{0, 0, 0, 0};
    u32x4 anf = {0u, 0u, 0u, 0u};
    unsigned consa = lane == 0 ? L_FLAG + 16 + w * 4 : L_SCR + 1024 + lane * 4;
    pin(anf); pin(consa);
    unsigned own_off[8];
#pragma unroll
    for (int f = 0; f < 8; ++f) { own_off[f] = f == member ? kOut : 0u; asm volatile("" : "+s"(own_off[f])); }
    asm volatile("" : "+v"(kv[0]), "+v"(kv[1]), "+v"(kv[2]), "+v"(kv[3]), "+v"(x), "+v"(a0), "+v"(va0), "+v"(va1));
    asm volatile("" : "+v"(v0[0][0]), "+v"(v0[0][1]), "+v"(v0[1][0]), "+v"(v0[1][1]), "+v"(v0[2][0]), "+v"(v0[2][1]), "+v"(v0[3][0]), "+v"(v0[3][1]),
                      "+v"(vA[0][0]), "+v"(vA[0][1]), "+v"(vA[1][0]), "+v"(vA[1][1]), "+v"(vA[2][0]), "+v"(vA[2][1]), "+v"(vA[3][0]), "+v"(vA[3][1]));
    asm volatile("" : "+v"(ea[0]), "+v"(ea[1]), "+v"(ea[2]), "+v"(ea[3]), "+v"(ea[4]), "+v"(ea[5]), "+v"(ea[6]), "+v"(ea[7]), "+v"(ks[0]), "+v"(ks[1]), "+v"(ks[2]), "+v"(ks[3]));
    wait_lds();
    flags_wait<0>(flags, 1);
    auto stage = [&](const int st, s16x4 (&vcur)[4][2], s16x4 (&vnxt)[4][2]) {
        const unsigned slot = st == 7 ? 0u : st * 16384u;
        const LAS char* kr0 = (const LAS char*)lds + slot + ra0; const LAS char* kr1 = (const LAS char*)lds + slot + ra1;
        const LAS char* vr0 = (const LAS char*)lds + slot + va0; const LAS char* vr1 = (const LAS char*)lds + slot + va1;
        u32x4 kf[8];
        float eb[8];
#pragma unroll
        for (int c = 0; c < 4; ++c) {
            kv[c] = mfma(asbf(a0), cat(v0[c][0], v0[c][1]), kv[c]);
            kf[2 * c] = *(const LAS u32x4*)(kr0 + 512 * c); kf[2 * c + 1] = *(const LAS u32x4*)(kr1 + 512 * c);
            eb[2 * c] = __builtin_amdgcn_exp2f(x[8 + 2 * c]); eb[2 * c + 1] = __builtin_amdgcn_exp2f(x[9 + 2 * c]);
            pin(eb[2 * c]); pin(eb[2 * c + 1]);
            fence();
        }
        u32x4 a1;
#pragma unroll
        for (int t = 0; t < 8; ++t) {
            x = mfma(asbf(kf[t]), asbf(pb[t]), t == 0 ? f32x16{} : x);
            s16x4 (&dst)[4][2] = t < 4 ? v0 : vnxt;
            dst[t & 3][0] = __builtin_amdgcn_ds_read_tr16_b64_v4i16((LAS s16x4*)(vr0 + (t >> 2) * 4096 + (t & 3) * 512));
            dst[t & 3][1] = __builtin_amdgcn_ds_read_tr16_b64_v4i16((LAS s16x4*)(vr1 + (t >> 2) * 4096 + (t & 3) * 512));
            if (t == 0) anf[0] = *(const LAS unsigned*)(lds + L_AN + st * 256 + lane * 4);
            if (t < 2) { unsigned u0 = pack2(eb[4 * t], eb[4 * t + 1]), u1 = pack2(eb[4 * t + 2], eb[4 * t + 3]); pin(u0); pin(u1); a1[2 * t] = u0; a1[2 * t + 1] = u1; }
            else {
                const int lo = t < 4 ? 2 * (t - 2) : 4 + 3 * (t - 4), hi = t < 4 ? lo + 2 : lo + 3;
#pragma unroll
                for (int i = lo; i < hi; ++i) { ks[i & 3] += i < 8 ? ea[i] : eb[i - 8]; pin(ks[i & 3]); }
            }
            if (t == 7) *(volatile LAS unsigned*)((LAS char*)lds + consa) = st + 1;
            fence();
        }
        x = mfma(asbf(anf), asbf(ones), x);
        fence();
        u32x4 fl;
#pragma unroll
        for (int c = 0; c < 4; ++c) {
            kv[c] = mfma(asbf(a1), cat(vcur[c][0], vcur[c][1]), kv[c]);
            if (c == 0) fl = *(const volatile LAS u32x4*)flags;
            if (c >= 1) {
#pragma unroll
                for (int i = 3 * (c - 1); i < (c == 3 ? 8 : 3 * c); ++i) { ea[i] = __builtin_amdgcn_exp2f(x[i]); pin(ea[i]); }
            }
            if (c == 2) { unsigned u0 = pack2(ea[0], ea[1]), u1 = pack2(ea[2], ea[3]); pin(u0); pin(u1); a0[0] = u0; a0[1] = u1; }
            fence();
        }
        a0[2] = pack2(ea[4], ea[5]); a0[3] = pack2(ea[6], ea[7]);
        pin(a0);
        if (st < 7 && min4(fl) < (unsigned)(st + 2)) flags_wait<0>(flags, st + 2);
        asm volatile("" ::: "memory");
        fence();
    };
#pragma unroll 1
    for (int st = 0; st < 8; st += 2) {
        stage(st, vA, vB);
        stage(st + 1, vB, vA);
    }
    {
        float eb[8];
#pragma unroll
        for (int c = 0; c < 4; ++c) {
            kv[c] = mfma(asbf(a0), cat(v0[c][0], v0[c][1]), kv[c]);
            eb[2 * c] = __builtin_amdgcn_exp2f(x[8 + 2 * c]); eb[2 * c + 1] = __builtin_amdgcn_exp2f(x[9 + 2 * c]);
            fence();
        }
        const u32x4 a1 = {pack2(eb[0], eb[1]), pack2(eb[2], eb[3]), pack2(eb[4], eb[5]), pack2(eb[6], eb[7])};
#pragma unroll
        for (int c = 0; c < 4; ++c) kv[c] = mfma(asbf(a1), cat(vA[c][0], vA[c][1]), kv[c]);
#pragma unroll
        for (int i = 0; i < 8; ++i) { ks[i & 3] += ea[i]; ks[(i + 2) & 3] += eb[i]; }
    }
    const unsigned pbase = head * WS_HEAD + WS_P + (member * 4 + w) * 8192 + lane * 16;
#pragma unroll
    for (int c = 0; c < 4; ++c)
#pragma unroll
        for (int s = 0; s < 2; ++s) __builtin_amdgcn_raw_buffer_store_b128(pack8(kv[c], s), wr, pbase + (c * 2 + s) * 1024 + own_off[c * 2 + s], 0, kSc1);
    const float kst = halfsum((ks[0] + ks[1]) + (ks[2] + ks[3]));
    if (h == 0) __builtin_amdgcn_raw_buffer_store_b32(__float_as_uint(kst), wr, head * WS_HEAD + WS_KS + member * 512 + (32 * w + r) * 4, 0, kSc1);
    f32x16 red = {};
#pragma unroll
    for (int f = 0; f < 8; ++f)
        if (f == member) {
#pragma unroll
            for (int i = 0; i < 8; ++i) red[i] = kv[f >> 1][8 * (f & 1) + i];
            asm volatile("" ::: "memory");
        }
    pin(red);
    u32x4 sel;
    {
        const int j = (r & 3) + 4 * (r >> 3);
        const bool on = r < 16 && ((r >> 2) & 1) == h;
#pragma unroll
        for (int i = 0; i < 4; ++i) sel[i] = on && (j >> 1) == i ? ((j & 1) ? 0x3f800000u : 0x00003f80u) : 0u;
    }
    unsigned po[8];
#pragma unroll
    for (int jj = 0; jj < 8; ++jj) { po[jj] = head * WS_HEAD + WS_P + (jj * 4 + w) * 8192 + member * 1024 + lane * 16 + own_off[jj]; pin(po[jj]); }
    unsigned ko = head * WS_HEAD + WS_KS + 4 * h * 512 + (32 * w + r) * 4;
    pin(ko); pin(sel);
    waitv<0>();
    tag += 1ull;
    if (lane == 0) __hip_atomic_store((gu64*)sync + member, tag, __ATOMIC_RELAXED, __HIP_MEMORY_SCOPE_AGENT);
#pragma unroll 1
    for (unsigned spins = 0; spins < kSpinCap; ++spins) {
        const unsigned long long f = __hip_atomic_load((const gu64*)sync + (lane & 7), __ATOMIC_RELAXED, __HIP_MEMORY_SCOPE_AGENT);
        if (__all(f == tag)) break;
        __builtin_amdgcn_s_sleep(1);
    }
    asm volatile("" ::: "memory");
    u32x4 part[8];
#pragma unroll
    for (int jj = 0; jj < 8; ++jj) part[jj] = __builtin_amdgcn_raw_buffer_load_b128(wr, po[jj], 0, kSc1);
    float kp[4];
#pragma unroll
    for (int i = 0; i < 4; ++i) kp[i] = __uint_as_float(__builtin_amdgcn_raw_buffer_load_b32(wr, ko + i * 512, 0, kSc1));
    fence();
#pragma unroll
    for (int jj = 0; jj < 8; ++jj) red = mfma(asbf(sel), asbf(part[jj]), red);
    u32x4 fin = pack8(red, 0);
#pragma unroll
    for (int i = 0; i < 4; ++i) fin[i] = fin[i] == kSent ? 0x7fc07fc0u : fin[i];
    __builtin_amdgcn_raw_buffer_store_b128(fin, wr, fbase + member * 1024, 0, kSc1);
    if (member == 0 && lane == 0) __hip_atomic_store((gu64*)(sync + 1024), tag, __ATOMIC_RELAXED, __HIP_MEMORY_SCOPE_AGENT);
    {
        const float kt = halfsum((kp[0] + kp[1]) + (kp[2] + kp[3]));
        const unsigned s2 = r >> 4, jhi = (r >> 3) & 1, hh = (r >> 2) & 1, jlo = r & 3;
        if (h == 0) *(LAS unsigned short*)(lds + L_KS + ((w * 2 + s2) * 2 + hh) * 16 + (4 * jhi + jlo) * 2) = (unsigned short)(pack2(kt, 0.f) & 0xffffu);
        flag_set(flags + 32 + w * 4, 1u, lane);
    }
    unsigned fb0 = fbase, fb1 = fbase + 4096;
    pin(fb0); pin(fb1);
    __builtin_amdgcn_s_sleep(18);
    u32x4 ff[8];
#pragma unroll 1
    for (unsigned spins = 0; spins < kSpinCap; ++spins) {
#pragma unroll
        for (int f = 0; f < 8; ++f) ff[f] = __builtin_amdgcn_raw_buffer_load_b128(wr, (f < 4 ? fb0 : fb1) + (f & 3) * 1024, 0, kSc1);
        fence();
        unsigned m = 0;
#pragma unroll
        for (int f = 0; f < 8; ++f) m = max(max(m, max(ff[f][0], ff[f][1])), max(ff[f][2], ff[f][3]));
        if (__all(m != kSent)) break;
        __builtin_amdgcn_s_sleep(1);
        asm volatile("" ::: "memory");
    }
#pragma unroll
    for (int f = 0; f < 8; ++f) *(LAS u32x4*)(lds + L_KV + (w * 8 + f) * 1024 + lane * 16) = ff[f];
    flag_set(flags + 48 + w * 4, 1u, lane);
    asm volatile(".Lperf_end_kv:");
}

extern "C" void kernel_launch(void* const* d_in, const int* in_sizes, int n_in, void* d_out, int out_size, void* d_ws, size_t ws_size,
                              hipStream_t stream) {
    performer_kernel<<<256, 512, 0, stream>>>((const char*)d_in[0], (const char*)d_in[1], (const char*)d_in[2], (const char*)d_in[3],
                                              (char*)d_out, (char*)d_ws);
}
```

```cpp
#include <hip/hip_runtime.h>
#include <hip/hip_bf16.h>
#include <stdint.h>
#include <utility>

typedef __bf16 bf16x2 __attribute__((ext_vector_type(2)));
typedef __bf16 bf16x8 __attribute__((ext_vector_type(8)));
typedef short s16x4 __attribute__((ext_vector_type(4)));
typedef float f32x2 __attribute__((ext_vector_type(2)));
typedef float f32x16 __attribute__((ext_vector_type(16)));
typedef unsigned u32x2 __attribute__((ext_vector_type(2)));
typedef unsigned u32x4 __attribute__((ext_vector_type(4)));
#define LAS __attribute__((address_space(3)))
#define GAS __attribute__((address_space(1)))
#define DI __device__ __forceinline__
typedef GAS unsigned long long gu64;

constexpr int kS = 2048;
constexpr float kC1 = 0.29730177875068026f * 1.4426950408889634f;
constexpr float kCN = -0.5f * 0.08838834764831845f * 1.4426950408889634f;
constexpr float kCB = -3.5f;

constexpr unsigned WS_HEAD = 0x100000;
constexpr unsigned WS_SYNC = 0;
constexpr unsigned WS_PRB = 0x10000;
constexpr unsigned WS_KS = 0x14000;
constexpr unsigned WS_F = 0x20000;
constexpr unsigned WS_P = 0x40000;
constexpr unsigned WS_BYTES = 32 * WS_HEAD;
constexpr unsigned kHalf = 8192;

constexpr unsigned L_Q = 1 * 16384;
constexpr unsigned L_KV = 5 * 16384;
constexpr unsigned L_KS = 7 * 16384;
constexpr unsigned L_FLAG = 7 * 16384 + 256;
constexpr unsigned L_VOTE = 7 * 16384 + 512;
constexpr unsigned L_AN = 7 * 16384 + 1024;
constexpr unsigned L_SCR = 7 * 16384 + 8192;
constexpr unsigned L_PROJ = 8 * 16384;
constexpr unsigned L_BYTES = 10 * 16384;

constexpr unsigned kSpinCap = 1u << 20;
constexpr unsigned kSent = 0xffffffffu;
constexpr unsigned kOut = 0x40000000u;
constexpr int kSc1 = 16, kNt = 2;

DI f32x16 mfma(bf16x8 a, bf16x8 b, f32x16 c) { return __builtin_amdgcn_mfma_f32_32x32x16_bf16(a, b, c, 0, 0, 0); }
DI unsigned pack2(float lo, float hi) { f32x2 f = {lo, hi}; return __builtin_bit_cast(unsigned, __builtin_convertvector(f, bf16x2)); }
DI float bflo(unsigned u) { return __uint_as_float(u << 16); }
DI float bfhi(unsigned u) { return __uint_as_float(u & 0xffff0000u); }
DI float sq2(unsigned u, float acc) { bf16x2 a = __builtin_bit_cast(bf16x2, u); return __builtin_amdgcn_fdot2_f32_bf16(a, a, acc, false); }
DI float halfsum(float x) { auto r = __builtin_amdgcn_permlane32_swap(__float_as_uint(x), __float_as_uint(x), false, false); return __uint_as_float(r[0]) + __uint_as_float(r[1]); }
DI bf16x8 asbf(u32x4 v) { return __builtin_bit_cast(bf16x8, v); }
DI bf16x8 cat(s16x4 lo, s16x4 hi) { return __builtin_bit_cast(bf16x8, __builtin_shufflevector(lo, hi, 0, 1, 2, 3, 4, 5, 6, 7)); }
DI u32x4 pack8(const f32x16& x, int s) {
    return u32x4{pack2(x[8 * s], x[8 * s + 1]), pack2(x[8 * s + 2], x[8 * s + 3]), pack2(x[8 * s + 4], x[8 * s + 5]), pack2(x[8 * s + 6], x[8 * s + 7])};
}
DI u32x4 norm_frag(float n, bool bias) {
    const float c = n * kCN + (bias ? kCB : 0.f);
    const unsigned hi = pack2(c, 0.f) & 0xffffu;
    return u32x4{pack2(bflo(hi), c - bflo(hi)), 0u, 0u, 0u};
}
template <int CTRL> DI float dppf(float x) { return __int_as_float(__builtin_amdgcn_update_dpp(0, __float_as_int(x), CTRL, 0xf, 0xf, true)); }
template <int... I, class F> DI void static_for_impl(std::integer_sequence<int, I...>, F&& f) { (f(std::integral_constant<int, I>{}), ...); }
template <int N, class F> DI void static_for(F&& f) { static_for_impl(std::make_integer_sequence<int, N>{}, f); }

DI void fence() { __builtin_amdgcn_sched_barrier(0); }
template <class T> DI void pin(T& x) { asm volatile("" : "+v"(x)); }
DI void wait_lds() { __builtin_amdgcn_s_waitcnt(0xc07f); asm volatile("" ::: "memory"); }
template <int N> DI void waitv() { asm volatile("s_waitcnt vmcnt(%0)" :: "i"(N) : "memory"); }

DI unsigned min4(u32x4 f) { return __builtin_amdgcn_readfirstlane(min(min(f[0], f[1]), min(f[2], f[3]))); }
template <int SLEEP> DI void flags_wait(const LAS char* p, unsigned need) {
#pragma unroll 1
    for (unsigned spins = 0; spins < kSpinCap; ++spins) {
        if (min4(*(const volatile LAS u32x4*)p) >= need) break;
        if (SLEEP) __builtin_amdgcn_s_sleep(SLEEP);
    }
    asm volatile("" ::: "memory");
}
DI void flag_set(LAS char* p, unsigned v, int lane) {
    asm volatile("" ::: "memory");
    if (lane == 0) *(volatile LAS unsigned*)p = v;
}

template <bool STREAM> DI void dma_rows(unsigned ldsaddr, unsigned voff, const char* base) {
    unsigned keep;
    if (STREAM)
        asm volatile("s_mov_b32 %0, m0\n\ts_mov_b32 m0, %1\n\ts_nop 0\n\tglobal_load_lds_dwordx4 %2, %3 offset:0 nt\n\t"
                     "s_add_u32 m0, %1, 896\n\ts_nop 0\n\tglobal_load_lds_dwordx4 %2, %3 offset:128 nt\n\ts_mov_b32 m0, %0"
                     : "=&s"(keep) : "s"(ldsaddr), "v"(voff), "s"(base) : "memory", "scc");
    else
        asm volatile("s_mov_b32 %0, m0\n\ts_mov_b32 m0, %1\n\ts_nop 0\n\tglobal_load_lds_dwordx4 %2, %3 offset:0\n\t"
                     "s_add_u32 m0, %1, 896\n\ts_nop 0\n\tglobal_load_lds_dwordx4 %2, %3 offset:128\n\ts_mov_b32 m0, %0"
                     : "=&s"(keep) : "s"(ldsaddr), "v"(voff), "s"(base) : "memory", "scc");
}

DI unsigned load_cycles(const char* p) {
    unsigned long long t0, t1; unsigned x;
    asm volatile("s_waitcnt lgkmcnt(0)\n\ts_memtime %0\n\ts_waitcnt lgkmcnt(0)\n\ts_load_dword %2, %3, 0x0\n\ts_waitcnt lgkmcnt(0)\n\ts_memtime %1\n\ts_waitcnt lgkmcnt(0)"
                 : "=&s"(t0), "=&s"(t1), "=&s"(x) : "s"(p) : "memory");
    return min((unsigned)(t1 - t0), 0xffffu);
}

__global__ __launch_bounds__(512, 2) void performer_kernel(const char* __restrict__ gq, const char* __restrict__ gk, const char* __restrict__ gv,
                                                           const char* __restrict__ gproj, char* gout, char* ws) {
    __shared__ __attribute__((aligned(16))) char lds[L_BYTES];
    const int lane = threadIdx.x & 63, r = lane & 31, h = lane >> 5;
    const int wv = __builtin_amdgcn_readfirstlane(threadIdx.x >> 6), w = wv & 3;
    const int b = blockIdx.x, head = (b & 7) * 4 + (b >> 6), member = (b >> 3) & 7;
    const size_t rowbase = ((size_t)head * kS + member * 256) * 256;
    const unsigned ldsbase = (unsigned)(size_t)(LAS char*)lds;
    {
        unsigned long long pc; unsigned len, len2, keep;
        asm volatile("s_getpc_b64 %0\n.Lperf_here:\n\ts_mov_b32 %1, .Lperf_end_q-.Lperf_here\n\ts_mov_b32 %2, .Lperf_end_kv-.Lperf_here" : "=s"(pc), "=s"(len), "=s"(len2));
        len = max(len, len2);
        const unsigned off = min((unsigned)((wv * 64 + lane) * 128), len - 4u) & ~3u;
        if (wv * 8192 < (int)len)
            asm volatile("s_mov_b32 %0, m0\n\ts_mov_b32 m0, %1\n\ts_nop 0\n\tglobal_load_lds_dword %2, %3\n\ts_mov_b32 m0, %0"
                         : "=&s"(keep) : "s"(ldsbase + L_SCR + wv * 256), "v"(off), "s"(pc) : "memory");
    }
    const unsigned lrow = (lane >> 2) & 7, cg = lane >> 5, sl = lane & 3, xb = (lane >> 4) & 1;
    unsigned dve = lrow * 256 + cg * 64 + 16 * (sl ^ xb), dvo = lrow * 256 + cg * 64 + 16 * (sl ^ (2 + xb));
    unsigned vo = w * 2048 + ((w & 1) ? dvo : dve);
    pin(dve); pin(dvo); pin(vo);

    asm volatile("" :: "s"(gq), "s"(gk), "s"(gv), "s"(gproj), "s"(gout), "s"(ws));
    const char* kb = gk + rowbase; const char* vb = gv + rowbase; const char* qb = gq + rowbase;
    const unsigned lk = __builtin_amdgcn_readfirstlane(ldsbase + w * 2048);
    auto kvg = [&](int st) { const unsigned slot = (st % 7) * 16384; dma_rows<true>(lk + slot, vo + st * 8192, kb); dma_rows<true>(lk + slot + 8192, vo + st * 8192, vb); };
    char* sync = ws + head * WS_HEAD + WS_SYNC + w * 16384;
    const __amdgpu_buffer_rsrc_t wr = __builtin_amdgcn_make_buffer_rsrc(ws, 0, WS_BYTES, 0x00020000);
    const unsigned fbase = head * WS_HEAD + WS_F + w * 16384 + lane * 16;
    if (wv >= 4) { kvg(0); kvg(1); }
    else {
        const unsigned lp = __builtin_amdgcn_readfirstlane(ldsbase + L_PROJ + w * 8192);
#pragma unroll
        for (int rg = 0; rg < 4; ++rg) dma_rows<false>(lp + rg * 2048, w * 8192 + rg * 2048 + ((rg & 1) ? dvo : dve), gproj);
        __builtin_amdgcn_raw_buffer_store_b128(u32x4{kSent, kSent, kSent, kSent}, wr, fbase + member * 1024, 0, kSc1);
        __builtin_amdgcn_raw_buffer_store_b128(u32x4{kSent, kSent, kSent, kSent}, wr, fbase + member * 1024 + kHalf, 0, kSc1);
        __builtin_amdgcn_raw_buffer_store_b32(0u, wr, lane < 4 ? head * WS_HEAD + WS_PRB + (member * 4 + w) * 256 + (lane & 1) * kHalf + (lane >> 1) * 128 : kOut, 0, kSc1);
    }
    unsigned long long tag = __hip_atomic_load((gu64*)(sync + 1024), __ATOMIC_RELAXED, __HIP_MEMORY_SCOPE_AGENT);
    LAS char* const flags = (LAS char*)lds + L_FLAG;
    if (lane < 8) *(volatile LAS unsigned*)(lane < 6 ? flags + lane * 16 + w * 4 : (LAS char*)lds + L_VOTE + (lane - 6) * 16 + w * 4) = lane < 6 ? 0u : ~0u;
    asm volatile("s_waitcnt lgkmcnt(0)\n\ts_barrier" ::: "memory");
    const unsigned ra0 = 2048 * (r >> 3) + 64 * (r & 7) + 16 * (h ^ ((r >> 2) & 3));
    const unsigned ra1 = 2048 * (r >> 3) + 64 * (r & 7) + 16 * ((2 + h) ^ ((r >> 2) & 3));
    const u32x4 ones = {0x3f803f80u, 0u, 0u, 0u};
    int role = wv;
    asm volatile("" : "+s"(role));

    if (role >= 4) {
        const unsigned lq = __builtin_amdgcn_readfirstlane(ldsbase + L_Q + w * 16384);
        auto qg = [&](int i) { dma_rows<true>(lq + i * 4096, w * 16384 + i * 4096 + dve, qb); dma_rows<true>(lq + i * 4096 + 2048, w * 16384 + i * 4096 + 2048 + dvo, qb); };
        const unsigned nra = w * 2048 + (lane >> 3) * 64 + ((lane & 7) >> 1) * 512 + (lane & 1) * 32;
        const unsigned nwa = L_AN + ((lane & 7) == 0 ? 0 : 128) + (w * 8 + (lane >> 3)) * 4;
        auto norms = [&](int st) {
            const LAS char* p = (const LAS char*)lds + (st % 7) * 16384 + nra;
            const u32x4 a = *(const LAS u32x4*)p, c = *(const LAS u32x4*)(p + 16);
            float n0 = sq2(a[0], 0.f), n1 = sq2(a[1], 0.f);
            n0 = sq2(a[2], n0); n1 = sq2(a[3], n1); n0 = sq2(c[0], n0); n1 = sq2(c[1], n1); n0 = sq2(c[2], n0); n1 = sq2(c[3], n1);
            float n = n0 + n1;
            n += dppf<0xB1>(n); n += dppf<0x4E>(n); n += dppf<0x141>(n);
            const unsigned v = norm_frag(n, true)[0];
            if ((lane & 7) < 2) *(LAS unsigned*)(lds + nwa + st * 256) = (lane & 7) == 0 ? v : 0u;
            asm volatile("" ::: "memory");
        };
        auto word = [&](int off, int v) { if (lane == 0) *(volatile LAS unsigned*)(flags + off + w * 4) = v; };
        auto issue = [&](int g) {
            if (g == 7) flags_wait<0>(flags + 16, 1);
            if (g == 8) flags_wait<0>(flags + 16, w + 2);
            if (g < 8) kvg(g); else qg(g - 8);
        };
        static_for<8>([&](auto Sc) {
            constexpr int st = Sc;
            waitv<4>();
            word(0, st + 1);
            if constexpr (st < 6) { issue(st + 2); norms(st); word(64, st + 1); }
            else { norms(st); word(64, st + 1); issue(st + 2); }
        });
        waitv<4>(); issue(10);
        waitv<4>(); issue(11);
        waitv<0>();
        const LAS char* qs = (const LAS char*)lds + L_Q + w * 16384;
        unsigned pjo = L_PROJ + lane * 16; pin(pjo);
        const LAS char* pj = (const LAS char*)lds + pjo;
        u32x4 qf[2][8];
#pragma unroll
        for (int u = 0; u < 2; ++u)
#pragma unroll
            for (int t = 0; t < 8; ++t) qf[u][t] = *(const LAS u32x4*)(qs + u * 8192 + ((t & 1) ? ra1 : ra0) + 512 * (t >> 1));
        u32x4 pa[4];
#pragma unroll
        for (int i = 0; i < 3; ++i) pa[i] = *(const LAS u32x4*)(pj + i * 1024);
        u32x4 an[2];
#pragma unroll
        for (int u = 0; u < 2; ++u) {
            float n0 = 0.f, n1 = 0.f;
#pragma unroll
            for (int t = 0; t < 8; ++t) { n0 = sq2(qf[u][t][0], n0); n1 = sq2(qf[u][t][1], n1); n0 = sq2(qf[u][t][2], n0); n1 = sq2(qf[u][t][3], n1); }
            an[u] = norm_frag(n0 + n1, h == 0);
        }
        fence();
        u32x4 pq[2][4][2];
        f32x16 x[2][2];
        static_for<72 + 18>([&](auto Gc) {
            constexpr int G = Gc, mt = G / 18, t = (G % 18) >> 1, u = G & 1;
            if constexpr (G < 72) {
                if constexpr (t == 0) x[mt & 1][u] = mfma(asbf(ones), asbf(an[u]), f32x16{});
                else {
                    constexpr int i = mt * 8 + t - 1;
                    if constexpr (u == 0 && i + 3 < 32) pa[(i + 3) & 3] = *(const LAS u32x4*)(pj + (i + 3) * 1024);
                    x[mt & 1][u] = mfma(asbf(pa[i & 3]), asbf(qf[u][t - 1]), x[mt & 1][u]);
                }
            }
            constexpr int E = G - 20;
            if constexpr (E >= 0 && E % 18 < 16 && E / 18 < 4) {
                constexpr int me = E / 18, pr = E % 18, ue = pr >> 3, k = pr & 7;
                const f32x16& p = x[me & 1][ue];
                unsigned v = pack2(__builtin_amdgcn_exp2f(p[2 * k]), __builtin_amdgcn_exp2f(p[2 * k + 1]));
                pin(v);
                pq[ue][me][k >> 2][k & 3] = v;
            }
            fence();
        });
        flags_wait<1>(flags + 32, 1);
        float rd[2];
        {
            u32x4 ka[8];
#pragma unroll
            for (int k = 0; k < 8; ++k) ka[k] = *(const LAS u32x4*)(lds + L_KS + k * 32 + h * 16);
            fence();
            f32x16 dn0 = {}, dn1 = {};
#pragma unroll
            for (int k = 0; k < 8; ++k) {
                dn0 = mfma(asbf(ka[k]), asbf(pq[0][k >> 1][k & 1]), dn0);
                dn1 = mfma(asbf(ka[k]), asbf(pq[1][k >> 1][k & 1]), dn1);
            }
            rd[0] = __builtin_amdgcn_rcpf(dn0[0] + 1e-6f); rd[1] = __builtin_amdgcn_rcpf(dn1[0] + 1e-6f);
            pin(rd[0]); pin(rd[1]);
        }
        const __amdgpu_buffer_rsrc_t orsrc = __builtin_amdgcn_make_buffer_rsrc(gout + rowbase, 0, 65536, 0x00020000);
        const unsigned gr = ((r & 7) << 1) | ((r >> 3) & 1);
        const unsigned rrow = lane >> 3, rch = lane & 7;
        unsigned rda = L_Q + w * 16384 + 128 * rrow + 16 * (rch ^ rrow);
        unsigned oo = (w * 64 + rrow) * 256 + rch * 16;
        unsigned sa[8];
#pragma unroll
        for (int i = 0; i < 8; ++i) { sa[i] = L_Q + w * 16384 + 128 * r + 8 * ((2 * i + h) ^ gr); pin(sa[i]); }
        unsigned fko = L_KV + lane * 16;
        pin(rda); pin(oo); pin(fko);
        const LAS char* fk = (const LAS char*)lds + fko;
        auto faddr = [&](int c, int k) { return fk + ((k >> 1) * 8 + c * 2 + (k & 1)) * 1024; };
        unsigned oswap;
        {
            flags_wait<1>(flags + 80, 1);
            const u32x4 v = *(const volatile LAS u32x4*)(lds + L_VOTE + 16);
            const unsigned m0 = min(min(v[0] & 0xffffu, v[1] & 0xffffu), min(v[2] & 0xffffu, v[3] & 0xffffu)), m1 = min(min(v[0] >> 16, v[1] >> 16), min(v[2] >> 16, v[3] >> 16));
            oswap = __builtin_amdgcn_readfirstlane(m0 < m1 ? kHalf : 0u);
            if (oswap) {
#pragma unroll
                for (int k = 0; k < 8; ++k) { const u32x4 t = pq[0][k >> 1][k & 1]; pq[0][k >> 1][k & 1] = pq[1][k >> 1][k & 1]; pq[1][k >> 1][k & 1] = t; }
                const float t = rd[0]; rd[0] = rd[1]; rd[1] = t;
                asm volatile("" ::: "memory");
            }
        }
        unsigned oo0 = oo ^ oswap, oo1 = (oo + kHalf) ^ oswap;
        pin(oo0); pin(oo1);
        flags_wait<1>(flags + 48, 1);
        u32x4 fa[2][8];
#pragma unroll
        for (int k = 0; k < 8; ++k) fa[0][k] = *(const LAS u32x4*)faddr(0, k);
        f32x16 o[3];
        u32x4 rb[4];
        unsigned pk[2];
        auto rb_read = [&](int uu, int hf, int i) { rb[i] = *(const LAS u32x4*)((const LAS char*)lds + rda + hf * 8192 + uu * 4096 + i * 1024); };
        auto rb_store = [&](int uu, int hf, int i) {
            const u32x4 t = rb[i];
            const u32x4 v = (i & 1) ? u32x4{t[2], t[3], t[0], t[1]} : t;
            __builtin_amdgcn_raw_buffer_store_b128(v, orsrc, (uu ? oo1 : oo0) + i * 8 * 256 + hf * 128, 0, kNt);
        };
        auto epi = [&](int jj, int k) {
            const int cp = 2 * (jj >> 2) + (jj & 1), uu = (jj >> 1) & 1;
            const f32x16& t = o[jj % 3];
            pk[k & 1] = pack2(t[2 * k] * rd[uu], t[2 * k + 1] * rd[uu]);
            if (k & 1) *(LAS u32x2*)((LAS char*)lds + sa[4 * (cp & 1) + (k >> 1)] + (cp >> 1) * 8192 + uu * 4096) = u32x2{pk[0], pk[1]};
            else pin(pk[0]);
        };
        static_for<74>([&](auto Gc) {
            constexpr int G = Gc, jj = G >> 3, k = G & 7, c = 2 * (jj >> 2) + (jj & 1), u = (jj >> 1) & 1;
            if constexpr (G < 64) {
                o[jj % 3] = mfma(asbf(fa[c & 1][k]), asbf(pq[u][k >> 1][k & 1]), k == 0 ? f32x16{} : o[jj % 3]);
                if constexpr (jj == 0) fa[1][k] = *(const LAS u32x4*)faddr(1, k);
                if constexpr (jj == 2 || jj == 3) fa[c & 1][k] = *(const LAS u32x4*)faddr(c + 2, k);
            }
            if constexpr (G >= 26 && G < 30) rb_read(0, 0, G - 26);
            if constexpr (G >= 30 && G < 34) rb_store(0, 0, G - 30);
            if constexpr (G >= 42 && G < 46) rb_read(1, 0, G - 42);
            if constexpr (G >= 46 && G < 50) rb_store(1, 0, G - 46);
            if constexpr (G >= 58 && G < 62) rb_read(0, 1, G - 58);
            if constexpr (G >= 62 && G < 66) rb_store(0, 1, G - 62);
            if constexpr (G >= 10) epi((G - 10) >> 3, (G - 10) & 7);
            fence();
        });
#pragma unroll
        for (int i = 0; i < 4; ++i) rb_read(1, 1, i);
#pragma unroll
        for (int i = 0; i < 4; ++i) rb_store(1, 1, i);
        asm volatile(".Lperf_end_q:");
        return;
    }

    waitv<4>();
    const char* prb = ws + head * WS_HEAD + WS_PRB + (member * 4 + w) * 256;
    unsigned lat[2] = {0xffffu, 0xffffu};
    auto probe = [&](int i) {
        const int hf = (w + i) & 1;
        const unsigned l = load_cycles(prb + hf * kHalf + (i >> 1) * 128);
        if (hf) lat[1] = min(lat[1], l); else lat[0] = min(lat[0], l);
    };
    u32x4 pb[8];
#pragma unroll
    for (int t = 0; t < 8; ++t) pb[t] = *(const LAS u32x4*)(lds + L_PROJ + w * 8192 + ((t & 1) ? ra1 : ra0) + 512 * (t >> 1));
    asm volatile("" : "+v"(pb[0]), "+v"(pb[1]), "+v"(pb[2]), "+v"(pb[3]), "+v"(pb[4]), "+v"(pb[5]), "+v"(pb[6]), "+v"(pb[7]));
#pragma unroll
    for (int t = 0; t < 8; ++t) {
#pragma unroll
        for (int i = 0; i < 4; ++i) pb[t][i] = pack2(bflo(pb[t][i]) * kC1, bfhi(pb[t][i]) * kC1);
        *(LAS u32x4*)(lds + L_PROJ + (w * 8 + t) * 1024 + lane * 16) = pb[t];
    }
    unsigned va0, va1;
    {
        const unsigned q = (lane & 15) >> 2, p = lane & 3, blk = (lane >> 4) & 1;
        va0 = 8192 + 64 * (4 * h + q) + 16 * ((2 * blk + (p >> 1)) ^ h) + 8 * (p & 1);
        va1 = 8192 + 2048 + 64 * (4 * h + q) + 16 * ((2 * blk + (p >> 1)) ^ (2 + h)) + 8 * (p & 1);
    }
    f32x16 kv[4] = {{}, {}, {}, {}};
    float ks[4] = {0.f, 0.f, 0.f, 0.f};
    f32x16 x;
#pragma unroll
    for (int i = 0; i < 16; ++i) x[i] = -1.0e30f;
    float ea[8] = {0.f, 0.f, 0.f, 0.f, 0.f, 0.f, 0.f, 0.f};
    u32x4 a0 = {0u, 0u, 0u, 0u};
    s16x4 v0[4][2], vA[4][2], vB[4][2];
#pragma unroll
    for (int i = 0; i < 8; ++i) v0[i >> 1][i & 1] = vA[i >> 1][i & 1] = s16x4{0, 0, 0, 0};
    u32x4 anf = {0u, 0u, 0u, 0u};
    unsigned consa = lane == 0 ? L_FLAG + 16 + w * 4 : L_SCR + 1024 + lane * 4;
    pin(anf); pin(consa);
    unsigned own_off[8];
#pragma unroll
    for (int f = 0; f < 8; ++f) { own_off[f] = f == member ? kOut : 0u; asm volatile("" : "+s"(own_off[f])); }
    asm volatile("" : "+v"(kv[0]), "+v"(kv[1]), "+v"(kv[2]), "+v"(kv[3]), "+v"(x), "+v"(a0), "+v"(va0), "+v"(va1));
    asm volatile("" : "+v"(v0[0][0]), "+v"(v0[0][1]), "+v"(v0[1][0]), "+v"(v0[1][1]), "+v"(v0[2][0]), "+v"(v0[2][1]), "+v"(v0[3][0]), "+v"(v0[3][1]),
                      "+v"(vA[0][0]), "+v"(vA[0][1]), "+v"(vA[1][0]), "+v"(vA[1][1]), "+v"(vA[2][0]), "+v"(vA[2][1]), "+v"(vA[3][0]), "+v"(vA[3][1]));
    asm volatile("" : "+v"(ea[0]), "+v"(ea[1]), "+v"(ea[2]), "+v"(ea[3]), "+v"(ea[4]), "+v"(ea[5]), "+v"(ea[6]), "+v"(ea[7]), "+v"(ks[0]), "+v"(ks[1]), "+v"(ks[2]), "+v"(ks[3]));
    wait_lds();
    probe(0);
    flags_wait<0>(flags, 1);
    auto stage = [&](const int st, s16x4 (&vcur)[4][2], s16x4 (&vnxt)[4][2]) {
        const unsigned slot = st == 7 ? 0u : st * 16384u;
        const LAS char* kr0 = (const LAS char*)lds + slot + ra0; const LAS char* kr1 = (const LAS char*)lds + slot + ra1;
        const LAS char* vr0 = (const LAS char*)lds + slot + va0; const LAS char* vr1 = (const LAS char*)lds + slot + va1;
        u32x4 kf[8];
        float eb[8];
#pragma unroll
        for (int c = 0; c < 4; ++c) {
            kv[c] = mfma(asbf(a0), cat(v0[c][0], v0[c][1]), kv[c]);
            kf[2 * c] = *(const LAS u32x4*)(kr0 + 512 * c); kf[2 * c + 1] = *(const LAS u32x4*)(kr1 + 512 * c);
            eb[2 * c] = __builtin_amdgcn_exp2f(x[8 + 2 * c]); eb[2 * c + 1] = __builtin_amdgcn_exp2f(x[9 + 2 * c]);
            pin(eb[2 * c]); pin(eb[2 * c + 1]);
            fence();
        }
        u32x4 a1, nf;
#pragma unroll
        for (int t = 0; t < 8; ++t) {
            x = mfma(asbf(kf[t]), asbf(pb[t]), t == 0 ? f32x16{} : x);
            s16x4 (&dst)[4][2] = t < 4 ? v0 : vnxt;
            dst[t & 3][0] = __builtin_amdgcn_ds_read_tr16_b64_v4i16((LAS s16x4*)(vr0 + (t >> 2) * 4096 + (t & 3) * 512));
            dst[t & 3][1] = __builtin_amdgcn_ds_read_tr16_b64_v4i16((LAS s16x4*)(vr1 + (t >> 2) * 4096 + (t & 3) * 512));
            if (t == 1) nf = *(const volatile LAS u32x4*)(flags + 64);
            if (t == 4) {
                if (min4(nf) < (unsigned)(st + 1)) flags_wait<0>(flags + 64, st + 1);
                asm volatile("" ::: "memory");
                anf[0] = *(const LAS unsigned*)(lds + L_AN + st * 256 + lane * 4);
            }
            if (t < 2) { unsigned u0 = pack2(eb[4 * t], eb[4 * t + 1]), u1 = pack2(eb[4 * t + 2], eb[4 * t + 3]); pin(u0); pin(u1); a1[2 * t] = u0; a1[2 * t + 1] = u1; }
            else {
                const int lo = t < 4 ? 2 * (t - 2) : 4 + 3 * (t - 4), hi = t < 4 ? lo + 2 : lo + 3;
#pragma unroll
                for (int i = lo; i < hi; ++i) { ks[i & 3] += i < 8 ? ea[i] : eb[i - 8]; pin(ks[i & 3]); }
            }
            if (t == 7) *(volatile LAS unsigned*)((LAS char*)lds + consa) = st + 1;
            fence();
        }
        x = mfma(asbf(anf), asbf(ones), x);
        fence();
        u32x4 fl;
#pragma unroll
        for (int c = 0; c < 4; ++c) {
            kv[c] = mfma(asbf(a1), cat(vcur[c][0], vcur[c][1]), kv[c]);
            if (c == 0) fl = *(const volatile LAS u32x4*)flags;
            if (c >= 1) {
#pragma unroll
                for (int i = 3 * (c - 1); i < (c == 3 ? 8 : 3 * c); ++i) { ea[i] = __builtin_amdgcn_exp2f(x[i]); pin(ea[i]); }
            }
            if (c == 2) { unsigned u0 = pack2(ea[0], ea[1]), u1 = pack2(ea[2], ea[3]); pin(u0); pin(u1); a0[0] = u0; a0[1] = u1; }
            fence();
        }
        a0[2] = pack2(ea[4], ea[5]); a0[3] = pack2(ea[6], ea[7]);
        pin(a0);
        if (st == 1 || st == 3) {
            probe(st == 1 ? 1 : 2);
            if (st == 3 && lane == 0) *(volatile LAS unsigned*)(lds + L_VOTE + w * 4) = lat[0] | lat[1] << 16;
            flags_wait<0>(flags, st + 2);
        } else if (st < 7 && min4(fl) < (unsigned)(st + 2)) flags_wait<0>(flags, st + 2);
        asm volatile("" ::: "memory");
        fence();
    };
#pragma unroll 1
    for (int st = 0; st < 8; st += 2) {
        stage(st, vA, vB);
        stage(st + 1, vB, vA);
    }
    unsigned half;
    {
        const u32x4 v = *(const volatile LAS u32x4*)(lds + L_VOTE);
        const unsigned m0 = min(min(v[0] & 0xffffu, v[1] & 0xffffu), min(v[2] & 0xffffu, v[3] & 0xffffu)), m1 = min(min(v[0] >> 16, v[1] >> 16), min(v[2] >> 16, v[3] >> 16));
        half = __builtin_amdgcn_readfirstlane(m1 < m0 ? 1u : 0u);
    }
    const unsigned hoff = half * kHalf;
    {
        float eb[8];
#pragma unroll
        for (int c = 0; c < 4; ++c) {
            kv[c] = mfma(asbf(a0), cat(v0[c][0], v0[c][1]), kv[c]);
            eb[2 * c] = __builtin_amdgcn_exp2f(x[8 + 2 * c]); eb[2 * c + 1] = __builtin_amdgcn_exp2f(x[9 + 2 * c]);
            fence();
        }
        const u32x4 a1 = {pack2(eb[0], eb[1]), pack2(eb[2], eb[3]), pack2(eb[4], eb[5]), pack2(eb[6], eb[7])};
#pragma unroll
        for (int c = 0; c < 4; ++c) kv[c] = mfma(asbf(a1), cat(vA[c][0], vA[c][1]), kv[c]);
#pragma unroll
        for (int i = 0; i < 8; ++i) { ks[i & 3] += ea[i]; ks[(i + 2) & 3] += eb[i]; }
    }
    const unsigned pbase = head * WS_HEAD + WS_P + (member * 4 + w) * 16384 + hoff + lane * 16;
#pragma unroll
    for (int c = 0; c < 4; ++c)
#pragma unroll
        for (int s = 0; s < 2; ++s) __builtin_amdgcn_raw_buffer_store_b128(pack8(kv[c], s), wr, pbase + (c * 2 + s) * 1024 + own_off[c * 2 + s], 0, kSc1);
    const float kst = halfsum((ks[0] + ks[1]) + (ks[2] + ks[3]));
    if (h == 0) __builtin_amdgcn_raw_buffer_store_b32(__float_as_uint(kst), wr, head * WS_HEAD + WS_KS + hoff + member * 512 + (32 * w + r) * 4, 0, kSc1);
    f32x16 red = {};
#pragma unroll
    for (int f = 0; f < 8; ++f)
        if (f == member) {
#pragma unroll
            for (int i = 0; i < 8; ++i) red[i] = kv[f >> 1][8 * (f & 1) + i];
            asm volatile("" ::: "memory");
        }
    pin(red);
    u32x4 sel;
    {
        const int j = (r & 3) + 4 * (r >> 3);
        const bool on = r < 16 && ((r >> 2) & 1) == h;
#pragma unroll
        for (int i = 0; i < 4; ++i) sel[i] = on && (j >> 1) == i ? ((j & 1) ? 0x3f800000u : 0x00003f80u) : 0u;
    }
    unsigned po[8], fo[8], ko[4];
#pragma unroll
    for (int jj = 0; jj < 8; ++jj) {
        po[jj] = head * WS_HEAD + WS_P + (jj * 4 + w) * 16384 + hoff + member * 1024 + lane * 16 + own_off[jj]; pin(po[jj]);
        fo[jj] = fbase + hoff + jj * 1024; pin(fo[jj]);
    }
#pragma unroll
    for (int i = 0; i < 4; ++i) { ko[i] = head * WS_HEAD + WS_KS + hoff + (4 * h + i) * 512 + (32 * w + r) * 4; pin(ko[i]); }
    pin(sel);
    waitv<0>();
    tag = (tag + 1ull) & 0x7fffffffffffffffull;
    if (lane < 2) __hip_atomic_store((gu64*)(sync + lane * kHalf) + member, tag << 1 | half, __ATOMIC_RELAXED, __HIP_MEMORY_SCOPE_AGENT);
    unsigned long long fw = 0;
#pragma unroll 1
    for (unsigned spins = 0; spins < kSpinCap; ++spins) {
        fw = __hip_atomic_load((const gu64*)(sync + hoff) + (lane & 7), __ATOMIC_RELAXED, __HIP_MEMORY_SCOPE_AGENT);
        if (__all(fw >> 1 == tag)) break;
        __builtin_amdgcn_s_sleep(1);
    }
    asm volatile("" ::: "memory");
    if (!__all(((unsigned)fw & 1u) == half)) {
        unsigned d[8];
#pragma unroll
        for (int jj = 0; jj < 8; ++jj) {
            d[jj] = ((__builtin_amdgcn_readlane((unsigned)fw, jj) & 1u) ^ half) * kHalf;
            po[jj] ^= d[jj]; fo[jj] ^= d[jj];
        }
#pragma unroll
        for (int i = 0; i < 4; ++i) ko[i] ^= h ? d[4 + i] : d[i];
    }
    u32x4 part[8];
#pragma unroll
    for (int jj = 0; jj < 8; ++jj) part[jj] = __builtin_amdgcn_raw_buffer_load_b128(wr, po[jj], 0, kSc1);
    float kp[4];
#pragma unroll
    for (int i = 0; i < 4; ++i) kp[i] = __uint_as_float(__builtin_amdgcn_raw_buffer_load_b32(wr, ko[i], 0, kSc1));
    fence();
#pragma unroll
    for (int jj = 0; jj < 8; ++jj) red = mfma(asbf(sel), asbf(part[jj]), red);
    u32x4 fin = pack8(red, 0);
#pragma unroll
    for (int i = 0; i < 4; ++i) fin[i] = fin[i] == kSent ? 0x7fc07fc0u : fin[i];
    __builtin_amdgcn_raw_buffer_store_b128(fin, wr, fbase + hoff + member * 1024, 0, kSc1);
    if (member == 0 && lane == 0) __hip_atomic_store((gu64*)(sync + 1024), tag, __ATOMIC_RELAXED, __HIP_MEMORY_SCOPE_AGENT);
    {
        const float kt = halfsum((kp[0] + kp[1]) + (kp[2] + kp[3]));
        const unsigned s2 = r >> 4, jhi = (r >> 3) & 1, hh = (r >> 2) & 1, jlo = r & 3;
        if (h == 0) *(LAS unsigned short*)(lds + L_KS + ((w * 2 + s2) * 2 + hh) * 16 + (4 * jhi + jlo) * 2) = (unsigned short)(pack2(kt, 0.f) & 0xffffu);
        flag_set(flags + 32 + w * 4, 1u, lane);
    }
    flags_wait<0>(flags + 16, 7);
    {
        unsigned lt[2];
#pragma unroll
        for (int i = 0; i < 2; ++i) { const int hf = (w + i) & 1; lt[hf] = load_cycles(gout + rowbase + w * 16384 + hf * kHalf); }
        if (lane == 0) *(volatile LAS unsigned*)(lds + L_VOTE + 16 + w * 4) = lt[0] | lt[1] << 16;
        flag_set(flags + 80 + w * 4, 1u, lane);
    }
    __builtin_amdgcn_s_sleep(4);
    u32x4 ff[8];
#pragma unroll 1
    for (unsigned spins = 0; spins < kSpinCap; ++spins) {
#pragma unroll
        for (int f = 0; f < 8; ++f) ff[f] = __builtin_amdgcn_raw_buffer_load_b128(wr, fo[f], 0, kSc1);
        fence();
        unsigned m = 0;
#pragma unroll
        for (int f = 0; f < 8; ++f) m = max(max(m, max(ff[f][0], ff[f][1])), max(ff[f][2], ff[f][3]));
        if (__all(m != kSent)) break;
        __builtin_amdgcn_s_sleep(1);
        asm volatile("" ::: "memory");
    }
#pragma unroll
    for (int f = 0; f < 8; ++f) *(LAS u32x4*)(lds + L_KV + (w * 8 + f) * 1024 + lane * 16) = ff[f];
    flag_set(flags + 48 + w * 4, 1u, lane);
    asm volatile(".Lperf_end_kv:");
}

extern "C" void kernel_launch(void* const* d_in, const int* in_sizes, int n_in, void* d_out, int out_size, void* d_ws, size_t ws_size,
                              hipStream_t stream) {
    performer_kernel<<<256, 512, 0, stream>>>((const char*)d_in[0], (const char*)d_in[1], (const char*)d_in[2], (const char*)d_in[3],
                                              (char*)d_out, (char*)d_ws);
}
```
